```python
import torch

def _softmax(x, axis=-1):
    return torch.softmax(x, dim=axis)
def _gelu(x):
    return torch.nn.functional.gelu(x, approximate="tanh")
def _silu(x):
    return torch.nn.functional.silu(x)
def _sigmoid(x):
    return torch.sigmoid(x)
def _layernorm(x, axis=-1, eps=1e-5):
    m = x.mean(dim=axis, keepdim=True)
    v = x.var(dim=axis, keepdim=True, unbiased=False)
    return (x - m) / torch.sqrt(v + eps)
def _scatter_add(tgt, idx, src, axis=0):
    return tgt.clone().index_add_(axis, idx, src)
def _segment_sum(src, seg, n):
    out = torch.zeros((n,) + src.shape[1:], dtype=src.dtype)
    return out.index_add_(0, seg, src)
def _pad(x, pw):
    flat = tuple(v for pair in reversed(pw) for v in pair)
    return torch.nn.functional.pad(x, flat)

kernel_name = 'gated_mix__ssm_conv_s4__swiglu_ffn__prompt'

def setup_inputs():

    _w = (lambda a: (lambda t: t.to(torch.bfloat16) if t.is_floating_point() else t)(torch.as_tensor(a)))
    def _A_setup():
        B, L, D, N = 1, 2048, 448, 256
        A_re = -(lambda _x: torch.exp(torch.as_tensor(_x)))((lambda *s: torch.randn(*s, dtype=torch.bfloat16) * 3.2)(D, N))
        A_im = torch.pi * (lambda *s: torch.randn(*s, dtype=torch.bfloat16) * 0.05)(D, N)
        B_re = (lambda *s: torch.randn(*s, dtype=torch.bfloat16) * 0.05)(D, N)
        B_im = (lambda *s: torch.randn(*s, dtype=torch.bfloat16) * 0.05)(D, N)
        C_re = (lambda *s: torch.randn(*s, dtype=torch.bfloat16) * 0.1)(D, N)
        C_im = (lambda *s: torch.randn(*s, dtype=torch.bfloat16) * 0.1)(D, N)
        log_dt = (lambda *s: torch.randn(*s, dtype=torch.bfloat16) * 0.05)(D) * 0.5 - 4.0
        Dskip = (lambda *s: torch.randn(*s, dtype=torch.bfloat16) * 0.05)(D)
        return {
            "u": _w((lambda *s: torch.randn(*s, dtype=torch.bfloat16) * 0.05)(B, L, D)),
            "A_re": _w(A_re),
            "A_im": _w(A_im),
            "B_re": _w(B_re),
            "B_im": _w(B_im),
            "C_re": _w(C_re),
            "C_im": _w(C_im),
            "log_dt": _w(log_dt),
            "Dskip": _w(Dskip),
        }
    _A_in = _A_setup()

    def _B_setup():
        B, S, D, F = 1, 2048, 448, 1792
        return {
            "x": _w((lambda *s: torch.randn(*s, dtype=torch.bfloat16) * 0.05)(B, S, D)),
            "w_gate": _w((lambda *s: torch.randn(*s, dtype=torch.bfloat16) * 0.05)(D, F)),
            "w_up": _w((lambda *s: torch.randn(*s, dtype=torch.bfloat16) * 0.05)(D, F)),
            "w_down": _w((lambda *s: torch.randn(*s, dtype=torch.bfloat16) * 0.05)(F, D)),
        }
    _B_in = _B_setup()

    return {
        "A_u": _A_in["u"],
        "A_A_re": _A_in["A_re"],
        "A_A_im": _A_in["A_im"],
        "A_B_re": _A_in["B_re"],
        "A_B_im": _A_in["B_im"],
        "A_C_re": _A_in["C_re"],
        "A_C_im": _A_in["C_im"],
        "A_log_dt": _A_in["log_dt"],
        "A_Dskip": _A_in["Dskip"],
        "B_x": _B_in["x"],
        "B_w_gate": _B_in["w_gate"],
        "B_w_up": _B_in["w_up"],
        "B_w_down": _B_in["w_down"],
        "gate_w": _w((lambda *s: torch.randn(*s, dtype=torch.bfloat16) * 0.05)(448) * 16.0),
    }

def reference(A_u, A_A_re, A_A_im, A_B_re, A_B_im, A_C_re, A_C_im, A_log_dt, A_Dskip, B_x, B_w_gate, B_w_up, B_w_down, gate_w):
    def _A_ref(u, A_re, A_im, B_re, B_im, C_re, C_im, log_dt, Dskip):
        Bsz, L, D = u.shape
        dt = torch.exp(log_dt)[:, None]
        A = A_re + 1j * A_im
        Bc = B_re + 1j * B_im
        Cc = C_re + 1j * C_im
        logdA = dt * A
        dB = dt * Bc
        l = (lambda x, dt: x.to(dt))(torch.arange(L), A_re.dtype)
        P = torch.exp(logdA[:, :, None] * l[None, None, :])
        K = torch.einsum("dn,dnl,dn->dl", Cc, P, dB).real
        L2 = 2 * L
        Kp = _pad(K, ((0, 0), (0, L)))
        up = (lambda x, *d: x.permute(*d))(u, 0, 2, 1)
        up = _pad(up, ((0, 0), (0, 0), (0, L)))
        Kf = (lambda x, axes=(-2,-1): torch.fft.rfft2(x.float(), dim=axes))(Kp[:, None, :], axes=(-2, -1))
        Uf = (lambda x, axes=(-2,-1): torch.fft.rfft2(x.float(), dim=axes))(up[:, :, None, :], axes=(-2, -1))
        Yf = Uf * Kf[None, :, :, :]
        y = (lambda x, s, axes=(-2,-1): torch.fft.irfft2(x, s=s, dim=axes).to(torch.bfloat16))(Yf, (1, L2), axes=(-2, -1))[:, :, 0, :L]
        y = (lambda x, *d: x.permute(*d))(y, 0, 2, 1) + u * Dskip[None, None, :]
        return y

    def _B_ref(x, w_gate, w_up, w_down):
        g = _silu(x @ w_gate)
        u = x @ w_up
        return (g * u) @ w_down

    x = A_u
    g = _sigmoid((x * gate_w).sum(-1, keepdims=True))
    a = _A_ref(x, A_A_re, A_A_im, A_B_re, A_B_im, A_C_re, A_C_im, A_log_dt, A_Dskip)
    b = _B_ref(x, B_w_gate, B_w_up, B_w_down)
    return g * a + (1 - g) * b
```

```cpp
#include <hip/hip_runtime.h>
#include <hip/hip_bf16.h>
typedef unsigned short u16;
__device__ __forceinline__ float b2f(u16 x){ return __uint_as_float(((unsigned)x)<<16); }
__device__ __forceinline__ u16 f2b(float f){ __hip_bfloat16 h = __float2bfloat16(f); return *(u16*)&h; }
__device__ __forceinline__ float rb(float f){ return b2f(f2b(f)); }
constexpr int L=2048, D=448, N=256, F=1792;

__global__ void kgen(const u16* Are,const u16* Aim,const u16* Bre,const u16* Bim,const u16* Cre,const u16* Cim,const u16* ldt,float* K){
  int d=blockIdx.y; int l=blockIdx.x*256+threadIdx.x;
  float dt=rb(expf(b2f(ldt[d])));
  float lf=rb((float)l);
  float acc=0;
  for(int n=0;n<N;n++){
    float ar=dt*b2f(Are[d*N+n]), ai=dt*b2f(Aim[d*N+n]);
    float br=dt*b2f(Bre[d*N+n]), bi=dt*b2f(Bim[d*N+n]);
    float cr=b2f(Cre[d*N+n]), ci=b2f(Cim[d*N+n]);
    float wr=cr*br-ci*bi, wi=cr*bi+ci*br;
    float e=expf(ar*lf); float s,c; sincosf(ai*lf,&s,&c);
    acc+=e*(wr*c-wi*s);
  }
  K[d*L+l]=acc;
}
__global__ void conv(const u16* u,const float* K,float* y){
  int d=blockIdx.y; int t=blockIdx.x*256+threadIdx.x;
  float acc=0;
  for(int s=0;s<=t;s++) acc+=K[d*L+t-s]*b2f(u[s*D+d]);
  y[t*D+d]=acc;
}
__global__ void g1(const u16* x,const u16* wg,const u16* wu,u16* h){
  int t=blockIdx.y; int f=blockIdx.x*256+threadIdx.x;
  float a=0,b=0;
  for(int k=0;k<D;k++){ float xv=b2f(x[t*D+k]); a+=xv*b2f(wg[k*F+f]); b+=xv*b2f(wu[k*F+f]); }
  a=rb(a); b=rb(b);
  float s=rb(a/(1.f+expf(-a)));
  h[t*F+f]=f2b(s*b);
}
__global__ void g2(const u16* x,const u16* h,const u16* wd,const float* y,const u16* dsk,const u16* gw,u16* out){
  int t=blockIdx.x; int d=threadIdx.x;
  float gs=0; for(int k=0;k<D;k++) gs+=rb(b2f(x[t*D+k])*b2f(gw[k]));
  gs=rb(gs); float g=rb(1.f/(1.f+expf(-gs)));
  float b=0; for(int k=0;k<F;k++) b+=b2f(h[t*F+k])*b2f(wd[k*D+d]);
  b=rb(b);
  float a=rb(rb(y[t*D+d])+rb(b2f(x[t*D+d])*b2f(dsk[d])));
  out[t*D+d]=f2b(rb(g*a)+rb(rb(1.f-g)*b));
}
extern "C" void kernel_launch(void* const* d_in, const int* in_sizes, int n_in, void* d_out, int out_size, void* d_ws, size_t ws_size, hipStream_t stream) {
  const u16* u=(const u16*)d_in[0];
  float* K=(float*)d_ws; float* y=K+D*L; u16* h=(u16*)(y+D*L);
  kgen<<<dim3(L/256,D),256,0,stream>>>((const u16*)d_in[1],(const u16*)d_in[2],(const u16*)d_in[3],(const u16*)d_in[4],(const u16*)d_in[5],(const u16*)d_in[6],(const u16*)d_in[7],K);
  conv<<<dim3(L/256,D),256,0,stream>>>(u,K,y);
  g1<<<dim3(F/256,L),256,0,stream>>>(u,(const u16*)d_in[10],(const u16*)d_in[11],h);
  g2<<<L,D,0,stream>>>(u,h,(const u16*)d_in[12],y,(const u16*)d_in[8],(const u16*)d_in[13],(u16*)d_out);
}
```

```cpp
#include <hip/hip_runtime.h>

typedef unsigned short u16;
typedef unsigned int u32;
typedef unsigned long long u64;
typedef __attribute__((ext_vector_type(8))) short s16x8;
typedef __attribute__((ext_vector_type(4))) short s16x4;
typedef __attribute__((ext_vector_type(2))) float f32x2;
typedef __attribute__((ext_vector_type(4))) float f32x4;
typedef __attribute__((ext_vector_type(16))) float f32x16;
typedef __attribute__((ext_vector_type(2))) u32 u32x2;
typedef __attribute__((ext_vector_type(4))) u32 u32x4;
typedef __attribute__((ext_vector_type(2))) __bf16 bf16x2;
#define LAS __attribute__((address_space(3)))
#define GAS __attribute__((address_space(1)))
#define DI __device__ __forceinline__
#define SB0 __builtin_amdgcn_sched_barrier(0)

namespace {
constexpr int L_ = 2048, D_ = 448, N_ = 256, F_ = 1792;
constexpr u32 WS_EPOCH = 0;
constexpr u32 WS_FU = 1024;
constexpr u32 WS_FH = WS_FU + 32 * 128;
constexpr u32 WS_FA = WS_FH + 224 * 128;
constexpr u32 WS_G = 65536;
constexpr u32 WS_UT = 131072;
constexpr u32 WS_AT = WS_UT + D_ * L_ * 2;
constexpr u32 WS_WD = WS_AT + D_ * L_ * 2;
constexpr u32 WS_H = WS_WD + F_ * D_ * 2;
constexpr u32 WS_END = WS_H + L_ * F_ * 2;
constexpr u32 WS_PROF = 16u << 20;
constexpr u32 LD_TAB = 131072;
constexpr u32 LD_KP = 147456;
constexpr u32 KP_STRIDE = 3088;
constexpr u32 LD_SYNC = 163584;
constexpr u32 CP_STRIDE = 35328;
constexpr u32 LD_UP = 70656, UP_STRIDE = 4672;
constexpr u32 LD_GP = 80128;
constexpr u32 SY_PAIR = LD_SYNC, SY_GDONE = LD_SYNC + 16, SY_HCNT = LD_SYNC + 20, SY_ACNT = LD_SYNC + 24,
              SY_CPDONE = LD_SYNC + 28;
constexpr u32 LD_SINK = LD_SYNC - 1024;
constexpr int SPIN_MAX = 1 << 16, SPIN_MAX_LDS = 1 << 20;
#define CODE_PREFETCH_LINES 0
#define KBARMASK ((1u << 0) | (1u << 3) | (1u << 5) | (1u << 8) | (1u << 10) | (1u << 13))

struct Params {
  const u16 *u, *are, *aim, *bre, *bim, *cre, *cim, *ldt, *dsk, *wg, *wu, *wd, *gw;
  u16* out;
  char* ws;
};

DI float bf_lo(u32 x) { return __uint_as_float(x << 16); }
DI float bf_hi(u32 x) { return __uint_as_float(x & 0xffff0000u); }
DI u32 pk_bf16(float a, float b) {
  f32x2 v = {a, b};
  return __builtin_bit_cast(u32, __builtin_convertvector(v, bf16x2));
}
DI u32 pk_bf16(f32x2 v) { return __builtin_bit_cast(u32, __builtin_convertvector(v, bf16x2)); }
DI f32x2 cmul(f32x2 a, f32x2 b) {
  f32x2 t, r;
  asm("v_pk_mul_f32 %0, %1, %2 op_sel_hi:[0,1]" : "=v"(t) : "v"(a), "v"(b));
  asm("v_pk_fma_f32 %0, %1, %2, %3 op_sel:[1,1,0] op_sel_hi:[1,0,1] neg_lo:[0,1,0]" : "=v"(r) : "v"(a), "v"(b), "v"(t));
  return r;
}
DI f32x2 cmul_conj(f32x2 a, f32x2 b) {
  f32x2 t, r;
  asm("v_pk_mul_f32 %0, %1, %2 op_sel_hi:[0,1] neg_hi:[0,1]" : "=v"(t) : "v"(a), "v"(b));
  asm("v_pk_fma_f32 %0, %1, %2, %3 op_sel:[1,1,0] op_sel_hi:[1,0,1] neg_lo:[0,1,0] neg_hi:[0,1,0]"
      : "=v"(r) : "v"(a), "v"(b), "v"(t));
  return r;
}
#define PKM " op_sel_hi:[0,1]\n\t"
#define PKF " op_sel:[1,1,0] op_sel_hi:[1,0,1] neg_lo:[0,1,0]\n\t"
#define PKMC " op_sel_hi:[0,1] neg_hi:[0,1]\n\t"
#define PKFC " op_sel:[1,1,0] op_sel_hi:[1,0,1] neg_lo:[0,1,0] neg_hi:[0,1,0]\n\t"
#define CM4(M, F) \
  asm("v_pk_mul_f32 %0, %4, %8" M "v_pk_mul_f32 %1, %5, %9" M "v_pk_mul_f32 %2, %6, %10" M "v_pk_mul_f32 %3, %7, %11" M \
      "v_pk_fma_f32 %0, %4, %8, %0" F "v_pk_fma_f32 %1, %5, %9, %1" F "v_pk_fma_f32 %2, %6, %10, %2" F "v_pk_fma_f32 %3, %7, %11, %3" F \
      : "=&v"(r[0]), "=&v"(r[1]), "=&v"(r[2]), "=&v"(r[3]) \
      : "v"(a[0]), "v"(a[1]), "v"(a[2]), "v"(a[3]), "v"(b[0]), "v"(b[1]), "v"(b[2]), "v"(b[3]))
DI void cmul4(f32x2 (&r)[4], const f32x2 (&a)[4], const f32x2 (&b)[4]) { CM4(PKM, PKF); }
DI void cmul4_conj(f32x2 (&r)[4], const f32x2 (&a)[4], const f32x2 (&b)[4]) { CM4(PKMC, PKFC); }
DI s16x8 pk4(const f32x2 (&r)[4]) {
  u32x4 o = {pk_bf16(r[0]), pk_bf16(r[1]), pk_bf16(r[2]), pk_bf16(r[3])};
  return __builtin_bit_cast(s16x8, o);
}
DI f32x2 cexp2pi(float x, float y) {
  float e = __builtin_amdgcn_exp2f(x);
  float f = __builtin_amdgcn_fractf(y);
  f32x2 r = {__builtin_amdgcn_cosf(f), __builtin_amdgcn_sinf(f)};
  return r * e;
}
template <int CTRL, int RM> DI float dpp_add(float x) {
  return x + __int_as_float(__builtin_amdgcn_update_dpp(0, __float_as_int(x), CTRL, RM, 0xf, true));
}
DI float wave_sum63(float x) {
  x = dpp_add<0x111, 0xf>(x); x = dpp_add<0x112, 0xf>(x); x = dpp_add<0x114, 0xf>(x); x = dpp_add<0x118, 0xf>(x);
  x = dpp_add<0x142, 0xa>(x); x = dpp_add<0x143, 0xc>(x);
  return x;
}
DI __amdgpu_buffer_rsrc_t mkrs(const void* p, u32 bytes) {
  return __builtin_amdgcn_make_buffer_rsrc((void*)p, 0, bytes, 0x00020000);
}
DI void dma16(u32 ldsaddr, u32 voff, __amdgpu_buffer_rsrc_t rs, u32 soff) {
  asm volatile("s_mov_b32 m0, %0\n\ts_nop 0\n\tbuffer_load_dwordx4 %1, %2, %3 offen lds"
               :: "s"(ldsaddr), "v"(voff), "s"(rs), "s"(soff) : "memory");
}
DI u64 ld_flag(const char* p) { return __hip_atomic_load((const GAS u64*)p, __ATOMIC_RELAXED, __HIP_MEMORY_SCOPE_AGENT); }
DI void st_flag(char* p, u64 v) { __hip_atomic_store((GAS u64*)p, v, __ATOMIC_RELAXED, __HIP_MEMORY_SCOPE_AGENT); }
DI void wait_flags(const char* p, u64 tag) {
#pragma unroll 1
  for (int it = 0; it < SPIN_MAX; ++it) {
    if (__all(ld_flag(p) == tag)) break;
    __builtin_amdgcn_s_sleep(2);
  }
  asm volatile("" ::: "memory");
}
DI void lds_wait_ge(LAS char* lds, u32 off, u32 v) {
#pragma unroll 1
  for (int it = 0; it < SPIN_MAX_LDS; ++it) {
    u32 x = __builtin_amdgcn_readfirstlane(*(volatile LAS u32*)(lds + off));
    if (x >= v) break;
    __builtin_amdgcn_s_sleep(1);
  }
  asm volatile("" ::: "memory");
}
DI void pair_sync(LAS char* lds, int s, u32 epoch) {
  asm volatile("s_waitcnt lgkmcnt(0)" ::: "memory");
  *(volatile LAS u32*)(lds + SY_PAIR + 4 * s) = epoch;
  lds_wait_ge(lds, SY_PAIR + 4 * (s ^ 1), epoch);
}
DI u32 lds_add(LAS char* lds, u32 off, int lane) {
  u32 old = 0;
  if (lane == 0) old = __hip_atomic_fetch_add((LAS u32*)(lds + off), 1u, __ATOMIC_RELAXED, __HIP_MEMORY_SCOPE_WORKGROUP);
  return __builtin_amdgcn_readfirstlane(old);
}
DI s16x8 tr_frag(LAS char* lds, u32 a0, u32 a1) {
  s16x4 x = __builtin_amdgcn_ds_read_tr16_b64_v4i16((LAS s16x4*)(lds + a0));
  s16x4 y = __builtin_amdgcn_ds_read_tr16_b64_v4i16((LAS s16x4*)(lds + a1));
  return __builtin_shufflevector(x, y, 0, 1, 2, 3, 4, 5, 6, 7);
}

#define STAMP(i)

DI void helper_block(const Params& P, LAS char* lds, int s, int tid, int lane, int wave, u64 tag) {
  constexpr u32 RS = 928;
  const int r = tid >> 3, c8 = tid & 7;
  u32x4 xu[7], xw[7], gw[7];
  const u32x4* up = (const u32x4*)(P.u + (size_t)(64 * s + r) * D_) + c8;
#pragma unroll
  for (int i = 0; i < 7; ++i) xu[i] = up[8 * i];
  const bool dow = s < 28;
  const u32x4* wp = (const u32x4*)(P.wd + (size_t)(64 * (dow ? s : 0) + r) * D_) + c8;
#pragma unroll
  for (int i = 0; i < 7; ++i) xw[i] = wp[8 * i];
#pragma unroll
  for (int i = 0; i < 7; ++i) gw[i] = ((const u32x4*)P.gw)[c8 + 8 * i];
  STAMP(2);
  float gs = 0.f;
#pragma unroll
  for (int i = 0; i < 7; ++i) {
    *(LAS u32x4*)(lds + r * RS + 16 * (c8 + 8 * i)) = xu[i];
#pragma unroll
    for (int j = 0; j < 4; ++j) gs += bf_lo(xu[i][j]) * bf_lo(gw[i][j]) + bf_hi(xu[i][j]) * bf_hi(gw[i][j]);
  }
#pragma unroll
  for (int i = 0; i < 7; ++i) *(LAS u32x4*)(lds + 65536 + r * RS + 16 * (c8 + 8 * i)) = xw[i];
  gs += __shfl_xor(gs, 1);
  gs += __shfl_xor(gs, 2);
  gs += __shfl_xor(gs, 4);
  auto rws = mkrs(P.ws, WS_END);
  if (c8 == 0) {
    float g = __builtin_amdgcn_rcpf(1.f + __expf(-gs));
    __builtin_amdgcn_raw_buffer_store_b32(__float_as_uint(g), rws, WS_G + 4 * (64 * s + r), 0, 16);
  }
  STAMP(3);
  __syncthreads();
  STAMP(4);
  const int q = lane >> 4, a = (lane & 15) >> 2, p = lane & 3;
#pragma unroll
  for (int i = 0; i < 7; ++i) {
    int un = wave * 7 + i, dt = un >> 1, th = un & 1;
    u32 ad = (32 * th + 8 * q + a) * RS + 32 * dt + 8 * p;
    s16x8 v = tr_frag(lds, ad, ad + 4 * RS);
    __builtin_amdgcn_raw_buffer_store_b128(__builtin_bit_cast(u32x4, v), rws,
                                           WS_UT + (16 * dt + (lane & 15)) * (L_ * 2) + (64 * s + 32 * th + 8 * q) * 2, 0, 16);
  }
  if (dow) {
#pragma unroll
    for (int i = 0; i < 7; ++i) {
      int un = wave * 7 + i, dt = un >> 1, kh = un & 1;
      u32 ad = 65536 + (32 * kh + 16 * (q >> 1) + 4 * (q & 1) + a) * RS + 32 * dt + 8 * p;
      s16x8 v = tr_frag(lds, ad, ad + 8 * RS);
      __builtin_amdgcn_raw_buffer_store_b128(__builtin_bit_cast(u32x4, v), rws,
                                             WS_WD + (((dt >> 2) * 56 + 2 * s + kh) * 4 + (dt & 3)) * 1024 + lane * 16, 0, 16);
    }
  }
  STAMP(5);
  asm volatile("s_waitcnt vmcnt(0)" ::: "memory");
  STAMP(6);
  __syncthreads();
  if (tid == 0) st_flag(P.ws + WS_FU + 128 * s, tag);
  STAMP(1);
  if (s == 0 && wave == 0) {
    wait_flags(P.ws + WS_FU + 128 * (lane & 31), tag);
#pragma unroll
    for (int i = 0; i < 4; ++i) {
      int b = lane + 64 * i;
      wait_flags(P.ws + WS_FH + 128 * (b < 224 ? b : 0), tag);
    }
    if (lane == 0) st_flag(P.ws + WS_EPOCH, tag);
  }
}

struct G1Frag { s16x8 a[2][2]; s16x8 b[2]; };

template <int C, int KS>
DI void g1_load(G1Frag& f, LAS char* lds, const u32 (&xa)[2][4], const u32 (&wa)[2][2][2]) {
  constexpr u32 sx = ((3 * C) & 7) * 16384u, sw = ((3 * C + 1 + (KS >> 1)) & 7) * 16384u + (KS & 1) * 4096u;
  constexpr u32 hx = sx >> 16, ox = sx & 0xffffu, hw = sw >> 16, ow = sw & 0xffffu;
#pragma unroll
  for (int tt = 0; tt < 2; ++tt) f.b[tt] = *(LAS s16x8*)(lds + xa[hx][KS] + ox + tt * 4096u);
#pragma unroll
  for (int g = 0; g < 2; ++g)
#pragma unroll
    for (int ft = 0; ft < 2; ++ft) f.a[g][ft] = tr_frag(lds, wa[hw][ft][0] + ow + g * 8192u, wa[hw][ft][1] + ow + g * 8192u);
}

struct G1Dma {
  __amdgpu_buffer_rsrc_t rx, rg, ru;
  u32 vx[2], vw[2];
  u32 wofs;
};
template <int U, int I>
DI void g1_dma(const G1Dma& d) {
  if constexpr (U < 21) {
    constexpr int c = U / 3, k = U % 3;
    constexpr u32 slot = (U & 7) * 16384u;
    if constexpr (k == 0) dma16(slot + d.wofs + I * 1024u, d.vx[I & 1], d.rx, (I >> 1) * 16u * 896u + c * 128u);
    else dma16(slot + (I >> 1) * 8192u + (d.wofs >> 1) + (I & 1) * 1024u, d.vw[I & 1], (I >> 1) ? d.ru : d.rg,
               (64u * c + 32u * (k - 1)) * 3584u);
  }
}
template <int U>
DI void g1_dma_unit(const G1Dma& d) { g1_dma<U, 0>(d); g1_dma<U, 1>(d); g1_dma<U, 2>(d); g1_dma<U, 3>(d); }

#define MF32(acc, a, b) acc = __builtin_amdgcn_mfma_f32_32x32x16_bf16(a, b, acc, 0, 0, 0)
#define MF16(acc, a, b) acc = __builtin_amdgcn_mfma_f32_16x16x32_bf16(a, b, acc, 0, 0, 0)

template <int C, int KS, int I>
DI void g1_rd(G1Frag& f, LAS char* lds, const u32 (&xa)[2][4], const u32 (&wa)[2][2][2]) {
  if constexpr (C < 7) {
    constexpr u32 sx = ((3 * C) & 7) * 16384u, sw = ((3 * C + 1 + (KS >> 1)) & 7) * 16384u + (KS & 1) * 4096u;
    constexpr u32 hx = sx >> 16, ox = sx & 0xffffu, hw = sw >> 16, ow = sw & 0xffffu;
    if constexpr (I == 0) {
      f.b[0] = *(LAS s16x8*)(lds + xa[hx][KS] + ox);
      f.b[1] = *(LAS s16x8*)(lds + xa[hx][KS] + ox + 4096u);
    } else {
      constexpr int g = (I - 1) >> 1, ft = (I - 1) & 1;
      f.a[g][ft] = tr_frag(lds, wa[hw][ft][0] + ow + g * 8192u, wa[hw][ft][1] + ow + g * 8192u);
    }
  }
}
template <int X>
DI void g1_piece(const G1Dma& d) {
  if constexpr (X >= 0) g1_dma<X / 4, X % 4>(d);
}
#define G1_PRO 8
constexpr int g1_slot_piece(int C, int KS, int J) {
  const int B = KS == 3 ? C + 1 : C, ph = KS == 3 ? 0 : KS + 1;
  int next = 4 * G1_PRO, res = -1;
  for (int c = 0; c < 7; ++c)
    for (int ks = 0; ks < 4; ++ks)
      for (int j = 0; j < 4; ++j) {
        const int b = ks == 3 ? c + 1 : c;
        const int lim = 4 * (8 + 3 * b);
        int p = -1;
        if (next < lim && next < 84) p = next++;
        if (c == C && ks == KS && j == J) res = p;
      }
  (void)B; (void)ph;
  return res;
}
constexpr int g1_issued_before(int C) {
  int n = 4 * G1_PRO;
  for (int c = 0; c <= C; ++c)
    for (int ks = 0; ks < (c == C ? 3 : 4); ++ks)
      for (int j = 0; j < 4; ++j) n += g1_slot_piece(c, ks, j) >= 0;
  return n;
}
template <int N> DI void wait_vm_barrier() { asm volatile("s_waitcnt vmcnt(%0) lgkmcnt(0)\n\ts_barrier" :: "n"(N) : "memory"); }

template <int C, int KS>
DI void g1_step(f32x16 (&acc)[2][2][2], G1Frag& cur, G1Frag& nxt, LAS char* lds, const u32 (&xa)[2][4],
                const u32 (&wa)[2][2][2], const G1Dma& d) {
  if constexpr (KS == 3 && C < 6) wait_vm_barrier<g1_issued_before(C) - 12 * (C + 2)>();
  constexpr int NC = KS == 3 ? C + 1 : C, NK = KS == 3 ? 0 : KS + 1;
  SB0;
  MF32(acc[0][0][0], cur.a[0][0], cur.b[0]); SB0;
  g1_rd<NC, NK, 0>(nxt, lds, xa, wa); SB0;
  MF32(acc[0][0][1], cur.a[0][0], cur.b[1]); SB0;
  g1_rd<NC, NK, 1>(nxt, lds, xa, wa);
  g1_piece<g1_slot_piece(C, KS, 0)>(d); SB0;
  MF32(acc[0][1][0], cur.a[0][1], cur.b[0]); SB0;
  g1_rd<NC, NK, 2>(nxt, lds, xa, wa); SB0;
  MF32(acc[0][1][1], cur.a[0][1], cur.b[1]); SB0;
  g1_rd<NC, NK, 3>(nxt, lds, xa, wa);
  g1_piece<g1_slot_piece(C, KS, 1)>(d); SB0;
  MF32(acc[1][0][0], cur.a[1][0], cur.b[0]); SB0;
  g1_rd<NC, NK, 4>(nxt, lds, xa, wa); SB0;
  MF32(acc[1][0][1], cur.a[1][0], cur.b[1]); SB0;
  g1_piece<g1_slot_piece(C, KS, 2)>(d); SB0;
  MF32(acc[1][1][0], cur.a[1][1], cur.b[0]); SB0;
  MF32(acc[1][1][1], cur.a[1][1], cur.b[1]); SB0;
  g1_piece<g1_slot_piece(C, KS, 3)>(d); SB0;
}
template <int C>
DI void g1_chunk(f32x16 (&acc)[2][2][2], G1Frag& f0, G1Frag& f1, LAS char* lds, const u32 (&xa)[2][4],
                 const u32 (&wa)[2][2][2], const G1Dma& d) {
  g1_step<C, 0>(acc, f0, f1, lds, xa, wa, d);
  g1_step<C, 1>(acc, f1, f0, lds, xa, wa, d);
  g1_step<C, 2>(acc, f0, f1, lds, xa, wa, d);
  g1_step<C, 3>(acc, f1, f0, lds, xa, wa, d);
}

}

__global__ __launch_bounds__(512, 2) void gated_mix_kernel(Params P) {
  __shared__ __attribute__((aligned(16))) char smem[163840];
  LAS char* lds = (LAS char*)smem;
  const int tid = threadIdx.x, lane = tid & 63;
  const int wave = __builtin_amdgcn_readfirstlane(tid >> 6);
  const int bid = blockIdx.x;
  STAMP(0);
  if (bid < 32) {
    const u64 tag = ld_flag(P.ws + WS_EPOCH) + 1;
    helper_block(P, lds, bid, tid, lane, wave, tag);
    return;
  }
  const int xcd = bid & 7, slot = (bid >> 3) - 4;
  const int tb = 4 * (xcd >> 1) + slot / 7, fb = 7 * (xcd & 1) + slot % 7;
  const int mid = tb * 14 + fb;
  const int rb = 4 * xcd + slot / 7, cb = slot % 7;
  auto rws = mkrs(P.ws, WS_END);
  s16x8 wdf[7][4];
  u64 tag;

  if (wave < 4) {
    G1Dma d;
    d.rx = mkrs(P.u + (size_t)tb * 128 * D_, 128 * D_ * 2);
    d.rg = mkrs(P.wg + fb * 128, (D_ * F_ - fb * 128) * 2);
    d.ru = mkrs(P.wu + fb * 128, (D_ * F_ - fb * 128) * 2);
    d.wofs = 4096u * wave;
#pragma unroll
    for (int i = 0; i < 2; ++i) {
      d.vx[i] = (32 * wave + 8 * i + (lane >> 3)) * 896 + 16 * ((lane & 7) ^ ((4 * i + (lane >> 4)) & 7));
      d.vw[i] = (8 * wave + 4 * i + (lane >> 4)) * 3584 + 16 * ((lane & 15) ^ (((lane >> 4) << 2) | ((2 * wave + i) & 3)));
    }
    g1_dma_unit<0>(d); g1_dma_unit<1>(d); g1_dma_unit<2>(d);
    if constexpr (G1_PRO > 3) g1_dma_unit<3>(d);
    if constexpr (G1_PRO > 4) g1_dma_unit<4>(d);
    if constexpr (G1_PRO > 5) g1_dma_unit<5>(d);
    if constexpr (G1_PRO > 6) g1_dma_unit<6>(d);
    if constexpr (G1_PRO > 7) g1_dma_unit<7>(d);
    const int wf = wave & 1, wt = wave >> 1, hh = lane >> 5;
    u32 xa[2][4], wa[2][2][2];
#pragma unroll
    for (int ks = 0; ks < 4; ++ks) {
      xa[0][ks] = 128 * (64 * wt + (lane & 31)) + 16 * ((2 * ks + hh) ^ ((lane >> 1) & 7));
      xa[1][ks] = xa[0][ks] + 65536;
      asm volatile("" : "+v"(xa[1][ks]));
    }
    {
      const int blk = (lane >> 4) & 1, q = (lane & 15) >> 2, p = lane & 3;
#pragma unroll
      for (int ft = 0; ft < 2; ++ft)
#pragma unroll
        for (int t = 0; t < 2; ++t) {
          wa[0][ft][t] = 256 * (8 * hh + 4 * t + q) + 16 * ((4 * (2 * wf + ft) + 2 * blk + (p >> 1)) ^ ((q << 2) | ((2 * hh + t) & 3))) + 8 * (p & 1);
          wa[1][ft][t] = wa[0][ft][t] + 65536;
          asm volatile("" : "+v"(wa[1][ft][t]));
        }
    }
    f32x16 acc[2][2][2] = {};
    G1Frag f0, f1;
    wait_vm_barrier<4 * G1_PRO - 12>();
    STAMP(1);
    g1_load<0, 0>(f0, lds, xa, wa);
    SB0;
    g1_chunk<0>(acc, f0, f1, lds, xa, wa, d);
    STAMP(2);
    g1_chunk<1>(acc, f0, f1, lds, xa, wa, d);
    g1_chunk<2>(acc, f0, f1, lds, xa, wa, d);
    g1_chunk<3>(acc, f0, f1, lds, xa, wa, d);
    STAMP(3);
    g1_chunk<4>(acc, f0, f1, lds, xa, wa, d);
    g1_chunk<5>(acc, f0, f1, lds, xa, wa, d);
    g1_chunk<6>(acc, f0, f1, lds, xa, wa, d);
    lds_add(lds, SY_GDONE, lane);
    __builtin_amdgcn_s_setprio(2);
    STAMP(4);
    tag = ld_flag(P.ws + WS_EPOCH) + 1;
    const u64 fu = ld_flag(P.ws + WS_FU + 128 * (lane & 31));
    const u32 hv = WS_H + ((u32)(tb * 8 + 4 * wt + ((lane >> 4) & 1)) * 56 + fb * 4 + 2 * wf) * 1024 + hh * 256 + (lane & 15) * 16;
#pragma unroll
    for (int ft = 0; ft < 2; ++ft)
#pragma unroll
      for (int tt = 0; tt < 2; ++tt)
#pragma unroll
        for (int s = 0; s < 2; ++s) {
          u32x4 o;
#pragma unroll
          for (int j = 0; j < 4; ++j) {
            f32x2 g = {acc[0][ft][tt][8 * s + 2 * j], acc[0][ft][tt][8 * s + 2 * j + 1]};
            f32x2 v = {acc[1][ft][tt][8 * s + 2 * j], acc[1][ft][tt][8 * s + 2 * j + 1]};
            f32x2 e = g * -1.4426950408889634f;
            e.x = __builtin_amdgcn_exp2f(e.x);
            e.y = __builtin_amdgcn_exp2f(e.y);
            e = e + 1.f;
            e.x = __builtin_amdgcn_rcpf(e.x);
            e.y = __builtin_amdgcn_rcpf(e.y);
            o[j] = pk_bf16(g * v * e);
          }
          __builtin_amdgcn_raw_buffer_store_b128(o, rws, hv + (tt * 2 * 56 + ft) * 1024 + s * 512, 0, 16);
        }
    STAMP(5);
    if (!__all(fu == tag)) wait_flags(P.ws + WS_FU + 128 * (lane & 31), tag);
    asm volatile("" ::: "memory");
#pragma unroll
    for (int k = 0; k < 7; ++k)
#pragma unroll
      for (int dt = 0; dt < 4; ++dt)
        wdf[k][dt] = __builtin_bit_cast(s16x8, __builtin_amdgcn_raw_buffer_load_b128(
            rws, lane * 16, WS_WD + ((cb * 56 + 7 * wave + k) * 4 + dt) * 1024, 16));
    asm volatile("s_waitcnt vmcnt(28)" ::: "memory");
    if (lds_add(lds, SY_HCNT, lane) == 3 && lane == 0) st_flag(P.ws + WS_FH + 128 * mid, tag);
    __builtin_amdgcn_s_setprio(0);
    STAMP(6);
  } else {
    __builtin_amdgcn_s_setprio(1);
    const int s = wave - 4, ch = s >> 1, sid = tid - 256, d0 = 2 * mid;
    const int eo = d0 * N_ + 2 * sid;
    const u32 p_are = *(const u32*)(P.are + eo), p_aim = *(const u32*)(P.aim + eo), p_bre = *(const u32*)(P.bre + eo),
              p_bim = *(const u32*)(P.bim + eo), p_cre = *(const u32*)(P.cre + eo), p_cim = *(const u32*)(P.cim + eo);
    const u32 p_ldt = P.ldt[d0 + ch], p_dsk = P.dsk[d0 + ch];
    tag = ld_flag(P.ws + WS_EPOCH) + 1;
    if (sid < 8) *(LAS u32*)(lds + LD_SYNC + 4 * sid) = 0;
    const int tl = lane + 64 * (s & 1);
    u32 kofs[8];
#pragma unroll
    for (int i = 0; i < 16; ++i) {
      const u32 l = 2047 - 8 * (tl + 128 * (i >> 3)) - (i & 7);
      const u32 v = (u32)bf_lo(pk_bf16((float)l, 0.f));
      const u32 o = (v & ~3u) + 16 * ((0xb1918100u >> (8 * (v & 3))) & 255u);
      if (i & 1) kofs[i >> 1] |= o << 16; else kofs[i >> 1] = o;
    }
    SB0;
    asm volatile("s_waitcnt lgkmcnt(0)\n\ts_barrier" ::: "memory");
    SB0;
    STAMP(1);
    LAS char* kp = lds + LD_KP + KP_STRIDE * s;
    {
      const float dt = bf_lo(pk_bf16(expf(bf_lo(p_ldt)), 0.f));
      float k2 = 0.f;
#pragma unroll
      for (int i = 0; i < 2; ++i) {
        const float ar = i ? bf_hi(p_are) : bf_lo(p_are), ai = i ? bf_hi(p_aim) : bf_lo(p_aim);
        const f32x2 B = {dt * (i ? bf_hi(p_bre) : bf_lo(p_bre)), dt * (i ? bf_hi(p_bim) : bf_lo(p_bim))};
        const f32x2 Cc = {i ? bf_hi(p_cre) : bf_lo(p_cre), i ? bf_hi(p_cim) : bf_lo(p_cim)};
        const float a2 = (dt * ar) * 1.4426950408889634f, b2 = (dt * ai) * 0.15915494309189535f;
        const f32x2 W = cmul(Cc, B);
        const f32x2 z1 = cexp2pi(a2, b2), zk = cexp2pi(1024.f * a2, 1024.f * b2);
        k2 += cmul(W, cmul(zk, zk)).x;
        f32x4 t0 = {a2, b2, W.x, W.y}, t1 = {z1.x, z1.y, zk.x, zk.y};
        *(LAS f32x4*)(lds + LD_TAB + 64 * sid + 32 * i) = t0;
        *(LAS f32x4*)(lds + LD_TAB + 64 * sid + 32 * i + 16) = t1;
      }
      k2 = wave_sum63(k2);
      if (lane == 63) *(LAS float*)(kp + 4 * 512) = k2;
      asm volatile("" :: "v"(tag));
    }
    STAMP(2);
    {
      f32x4 ka[5] = {};
      const float pw = 4.f * (lane & 15);
      u32 tab = LD_TAB + 4096 * s + 128 * (lane >> 4);
      asm volatile("" : "+v"(tab));
      f32x4 t0[4], t1[4];
#pragma unroll
      for (int i = 0; i < 4; ++i) t0[i] = *(LAS f32x4*)(lds + tab + 32 * i);
#pragma unroll 1
      for (int ks = 0; ks < 8; ++ks) {
        f32x2 z[4], w[4], y[4], c[4];
#pragma unroll
        for (int i = 0; i < 4; ++i) t1[i] = *(LAS f32x4*)(lds + tab + 32 * i + 16);
#pragma unroll
        for (int i = 0; i < 4; ++i) {
          const f32x2 xy = t0[i].xy * pw;
          z[i] = cexp2pi(xy.x, xy.y);
          c[i] = t0[i].zw;
        }
        cmul4_conj(w, z, c);
        tab += 512;
#pragma unroll
        for (int i = 0; i < 4; ++i) t0[i] = *(LAS f32x4*)(lds + tab + 32 * i);
        const s16x8 a = pk4(w);
        if ((KBARMASK >> (2 * ks)) & 1) asm volatile("s_barrier" ::: "memory");
        cmul4(y, z, z); cmul4(z, y, y); cmul4(y, z, z); cmul4(z, y, y);
        MF16(ka[0], a, pk4(z));
#pragma unroll
        for (int i = 0; i < 4; ++i) c[i] = t1[i].zw;
        cmul4(y, z, c);
        MF16(ka[4], a, pk4(y));
#pragma unroll
        for (int i = 0; i < 4; ++i) c[i] = t1[i].xy;
        cmul4(y, z, c);
        MF16(ka[1], a, pk4(y));
        cmul4(z, y, c);
        MF16(ka[2], a, pk4(z));
        cmul4(y, z, c);
        MF16(ka[3], a, pk4(y));
        if ((KBARMASK >> (2 * ks + 1)) & 1) asm volatile("s_barrier" ::: "memory");
      }
      const u32 o = 64 * (lane & 15) + 16 * (lane >> 4);
      *(LAS f32x4*)(kp + o) = ka[0];
      *(LAS f32x4*)(kp + 1024 + o) = ka[4];
      if ((lane & 15) < 8) *(LAS f32x4*)(kp + 4 * 580 + o) = ka[2];
      if ((lane & 15) < 4) {
        *(LAS f32x4*)(kp + 4 * 516 + o) = ka[1];
        *(LAS f32x4*)(kp + 4 * 708 + o) = ka[3];
      }
    }
    STAMP(3);
    wait_flags(P.ws + WS_FU + 128 * (lane & 31), tag);
    u32x4 urow[2];
#pragma unroll
    for (int i = 0; i < 2; ++i)
      urow[i] = __builtin_amdgcn_raw_buffer_load_b128(rws, 16 * tl, WS_UT + (d0 + ch) * (L_ * 2) + 2048 * i, 16);
#pragma unroll
    for (int k = 0; k < 7; ++k)
#pragma unroll
      for (int dt = 0; dt < 4; ++dt)
        wdf[k][dt] = __builtin_bit_cast(s16x8, __builtin_amdgcn_raw_buffer_load_b128(
            rws, lane * 16, WS_WD + ((cb * 56 + 7 * wave + k) * 4 + dt) * 1024, 16));
    pair_sync(lds, s, 1);
    LAS char* rk = lds + LD_TAB + 8192 * ch;
    {
      LAS char* k0 = lds + LD_KP + KP_STRIDE * 2 * ch;
#pragma unroll
      for (int b = 0; b < 2; ++b) {
        u32x4 o;
#pragma unroll
        for (int j = 0; j < 4; ++j) {
          const u32 w = kofs[4 * b + j], lo = w & 0xffffu, hi = w >> 16;
          o[j] = pk_bf16(*(LAS float*)(k0 + lo) + *(LAS float*)(k0 + KP_STRIDE + lo),
                         *(LAS float*)(k0 + hi) + *(LAS float*)(k0 + KP_STRIDE + hi));
        }
        *(LAS u32x4*)(rk + 16 * (tl + 128 * b)) = o;
      }
      if (tl == 0) *(LAS u32x4*)(rk + 4096) = u32x4{0, 0, 0, 0};
    }
    pair_sync(lds, s, 2);
    STAMP(4);
    lds_wait_ge(lds, SY_GDONE, 4);
    STAMP(5);
    LAS char* cp = lds + CP_STRIDE * ch;
    LAS char* upl = lds + LD_UP + UP_STRIDE * ch;
    {
      const u32x4 z4 = {0, 0, 0, 0};
      *(LAS u32x4*)(cp + 16 * tl) = z4;
      if (tl < 24) *(LAS u32x4*)(cp + 2048 + 16 * tl) = z4;
      else if (tl < 32) *(LAS u32x4*)(cp + 275 * 128 + 16 * (tl - 24)) = z4;
      else if (tl < 64) *(LAS u32x4*)(upl + 16 * (tl - 32)) = z4;
      else if (tl < 68) *(LAS u32x4*)(upl + 4608 + 16 * (tl - 64)) = z4;
#pragma unroll
      for (int b = 0; b < 2; ++b) {
        const int be = tl + 128 * b;
        const u32x4 lo = *(LAS u32x4*)(rk + 16 * (255 - be)), hi = *(LAS u32x4*)(rk + 16 * (256 - be));
        const u32 dd[8] = {lo[0], lo[1], lo[2], lo[3], hi[0], hi[1], hi[2], hi[3]};
        u32 aa[7];
#pragma unroll
        for (int k = 0; k < 7; ++k) aa[k] = __builtin_amdgcn_alignbit(dd[k + 1], dd[k], 16);
        const u32 base = 128 * (be + 19) + 16 * ((be + 19) & 7);
#pragma unroll
        for (int sh = 0; sh < 8; ++sh) {
          const int o = (7 - sh) >> 1;
          const u32x4 v = (sh & 1) ? u32x4{dd[o], dd[o + 1], dd[o + 2], dd[o + 3]} : u32x4{aa[o], aa[o + 1], aa[o + 2], aa[o + 3]};
          *(LAS u32x4*)(cp + (base ^ (16 * sh))) = v;
        }
      }
#pragma unroll
      for (int i = 0; i < 2; ++i) *(LAS u32x4*)(upl + 512 + 2048 * i + 16 * tl) = urow[i];
    }
    pair_sync(lds, s, 3);
    lds_add(lds, SY_CPDONE, lane);
    STAMP(6);
    f32x4 ya[8] = {};
    {
      const int c = lane & 7, hi = (lane >> 3) & 1, q = lane >> 4;
      const u32 ub = 512 + 16 * ((lane & 15) + q);
#pragma unroll
      for (int i = 0; i < 4; ++i) {
        const bool up = (s & 1);
        const int rho = i < 3 ? (up ? 5 + i : i) : (up ? 3 : 4);
        const bool sft = i < 3 && up;
        const int bq = 16 * hi - q + 19 - 4 * rho + (sft ? 32 : 0);
        const u32 ab = 128 * bq + 16 * (c ^ (bq & 7));
        const u32 bb = ub + 64 * rho - (sft ? 512 : 0);
        s16x8 bf[8];
#pragma unroll
        for (int p = 0; p < 8; ++p) bf[p] = *(LAS s16x8*)(upl + bb + 512 * p);
#pragma unroll
        for (int e = 0; e < 8; ++e) {
          const s16x8 af = *(LAS s16x8*)(cp + ab + 4096 * e);
#pragma unroll
          for (int jt = e; jt < 8; ++jt) MF16(ya[jt], af, bf[jt - e]);
        }
        if (i == 3 && !up) {
          const s16x8 bm = *(LAS s16x8*)(upl + bb - 512);
#pragma unroll
          for (int jt = 0; jt < 8; ++jt) {
            const s16x8 af = *(LAS s16x8*)(cp + ab + 4096 * (jt + 1));
            MF16(ya[jt], af, bm);
          }
        }
      }
    }
    STAMP(7);
    pair_sync(lds, s, 4);
    {
      const bool up = (s & 1);
      f32x4 keep[4];
#pragma unroll
      for (int j = 0; j < 4; ++j) {
        f32x4 lo = ya[j], hi = ya[4 + j];
        asm volatile("" : "+v"(lo), "+v"(hi));
        keep[j] = up ? hi : lo;
        *(LAS f32x4*)(cp + 32768 - 8192 + 4096 * (s & 1) + 1024 * j + 16 * lane) = up ? lo : hi;
      }
      pair_sync(lds, s, 5);
      const float dsk = bf_lo(p_dsk);
      const int q = lane >> 4;
      const u32 tq = 128 * (q >> 1) + 8 * (lane & 15) + 4 * (q & 1) + (up ? 1024 : 0);
#pragma unroll
      for (int j = 0; j < 4; ++j) {
        f32x4 y = *(LAS f32x4*)(cp + 32768 - 8192 + 4096 * ((s & 1) ^ 1) + 1024 * j + 16 * lane) + keep[j];
        const u32x2 uu = *(LAS u32x2*)(upl + 512 + 2 * (tq + 256 * j));
        u32x2 o = {pk_bf16(y[0] + dsk * bf_lo(uu[0]), y[1] + dsk * bf_hi(uu[0])),
                   pk_bf16(y[2] + dsk * bf_lo(uu[1]), y[3] + dsk * bf_hi(uu[1]))};
        __builtin_amdgcn_raw_buffer_store_b64(o, rws, 2 * tq, WS_AT + (d0 + ch) * (L_ * 2) + 512 * j, 16);
      }
      pair_sync(lds, s, 6);
    }
    asm volatile("s_waitcnt vmcnt(0)" ::: "memory");
    __builtin_amdgcn_s_waitcnt(0x0F70);
    if (lds_add(lds, SY_ACNT, lane) == 3 && lane == 0) st_flag(P.ws + WS_FA + 128 * mid, tag);
    __builtin_amdgcn_s_setprio(0);
    STAMP(8);
  }

  {
    const int j0 = (7 * wave) >> 2, j1 = (7 * wave + 6) >> 2;
    const int jj = j0 + (lane & 3);
    wait_flags(P.ws + WS_FH + 128 * ((rb >> 1) * 14 + (jj < j1 ? jj : j1)), tag);
    STAMP(9);
    s16x8 hf[4][7];
    const u32 hs = WS_H + ((u32)rb * 4 * 56 + 7 * wave) * 1024;
#pragma unroll
    for (int tt = 0; tt < 3; ++tt)
#pragma unroll
      for (int k = 0; k < 7; ++k)
        hf[tt][k] = __builtin_bit_cast(s16x8, __builtin_amdgcn_raw_buffer_load_b128(rws, lane * 16, hs + (tt * 56 + k) * 1024, 16));
    const u32 pb = wave < 3 ? LD_GP + 16384 * wave : wave == 3 ? LD_TAB : CP_STRIDE * ((wave - 4) >> 1) + 16384 * (wave & 1);
    if (wave == 3) lds_wait_ge(lds, SY_CPDONE, 4);
    LAS char* pp = lds + pb + 256 * (lane & 15);
    SB0;
#pragma unroll
    for (int tt = 0; tt < 4; ++tt) {
      f32x4 oa[4] = {};
#pragma unroll
      for (int k = 0; k < 7; ++k)
#pragma unroll
        for (int dt = 0; dt < 4; ++dt) MF16(oa[dt], wdf[k][dt], hf[tt][k]);
      if (tt == 0) {
        SB0;
#pragma unroll
        for (int k = 0; k < 7; ++k)
          hf[3][k] = __builtin_bit_cast(s16x8, __builtin_amdgcn_raw_buffer_load_b128(rws, lane * 16, hs + (3 * 56 + k) * 1024, 16));
        SB0;
      }
#pragma unroll
      for (int dt = 0; dt < 4; ++dt) *(LAS f32x4*)(pp + 4096 * tt + 16 * ((4 * dt + (lane >> 4)) ^ (lane & 15))) = oa[dt];
    }
    STAMP(10);
    const int t = 64 * rb + 16 * (wave >> 1) + (lane & 15);
    const float g = __uint_as_float(__builtin_amdgcn_raw_buffer_load_b32(rws, 4 * t, WS_G, 16));
    if (wave < 4) {
      wait_flags(P.ws + WS_FA + 128 * (32 * cb + (lane & 31)), tag);
      lds_wait_ge(lds, SY_CPDONE, 4);
      u32x4 av[2];
#pragma unroll
      for (int i = 0; i < 2; ++i)
        av[i] = __builtin_amdgcn_raw_buffer_load_b128(rws, (u32)(64 * cb + (tid >> 3) + 32 * i) * (L_ * 2) + 16 * (tid & 7), WS_AT + 128 * rb, 16);
#pragma unroll
      for (int i = 0; i < 2; ++i) *(LAS u32x4*)(lds + LD_KP + 144 * ((tid >> 3) + 32 * i) + 16 * (tid & 7)) = av[i];
    }
    STAMP(11);
    asm volatile("s_waitcnt lgkmcnt(0)\n\ts_barrier" ::: "memory");
    STAMP(12);
    const int gi = lane >> 4, dh = wave & 1;
    f32x4 b0 = {}, b1 = {};
    const u32 ro = 4096 * (wave >> 1) + 256 * (lane & 15);
    const u32 r0 = ro + 16 * ((8 * dh + 2 * gi) ^ (lane & 15)), r1 = ro + 16 * ((8 * dh + 2 * gi + 1) ^ (lane & 15));
#pragma unroll
    for (int w = 0; w < 8; ++w) {
      const u32 o = w < 3 ? LD_GP + 16384 * w : w == 3 ? LD_TAB : CP_STRIDE * ((w - 4) >> 1) + 16384 * (w & 1);
      b0 += *(LAS f32x4*)(lds + o + r0);
      b1 += *(LAS f32x4*)(lds + o + r1);
    }
    const u32 aad = LD_KP + 144 * (32 * dh + 8 * gi + ((lane & 15) >> 2)) + 32 * (wave >> 1) + 8 * (lane & 3);
    const u32x4 av = __builtin_bit_cast(u32x4, tr_frag(lds, aad, aad + 4 * 144));
    const float og = 1.f - g;
    u32x4 o;
    o[0] = pk_bf16(g * bf_lo(av[0]) + og * b0[0], g * bf_hi(av[0]) + og * b0[1]);
    o[1] = pk_bf16(g * bf_lo(av[1]) + og * b0[2], g * bf_hi(av[1]) + og * b0[3]);
    o[2] = pk_bf16(g * bf_lo(av[2]) + og * b1[0], g * bf_hi(av[2]) + og * b1[1]);
    o[3] = pk_bf16(g * bf_lo(av[3]) + og * b1[2], g * bf_hi(av[3]) + og * b1[3]);
    *(u32x4*)(P.out + (size_t)t * D_ + 64 * cb + 32 * dh + 8 * gi) = o;
    STAMP(13);
  }
}

extern "C" void kernel_launch(void* const* d_in, const int* in_sizes, int n_in, void* d_out, int out_size, void* d_ws,
                              size_t ws_size, hipStream_t stream) {
  Params P;
  P.u = (const u16*)d_in[0];
  P.are = (const u16*)d_in[1];
  P.aim = (const u16*)d_in[2];
  P.bre = (const u16*)d_in[3];
  P.bim = (const u16*)d_in[4];
  P.cre = (const u16*)d_in[5];
  P.cim = (const u16*)d_in[6];
  P.ldt = (const u16*)d_in[7];
  P.dsk = (const u16*)d_in[8];
  P.wg = (const u16*)d_in[10];
  P.wu = (const u16*)d_in[11];
  P.wd = (const u16*)d_in[12];
  P.gw = (const u16*)d_in[13];
  P.out = (u16*)d_out;
  P.ws = (char*)d_ws;
  gated_mix_kernel<<<256, 512, 0, stream>>>(P);
}
```

```cpp
#include <hip/hip_runtime.h>

typedef unsigned short u16;
typedef unsigned int u32;
typedef unsigned long long u64;
typedef __attribute__((ext_vector_type(8))) short s16x8;
typedef __attribute__((ext_vector_type(4))) short s16x4;
typedef __attribute__((ext_vector_type(2))) float f32x2;
typedef __attribute__((ext_vector_type(4))) float f32x4;
typedef __attribute__((ext_vector_type(16))) float f32x16;
typedef __attribute__((ext_vector_type(2))) u32 u32x2;
typedef __attribute__((ext_vector_type(4))) u32 u32x4;
typedef __attribute__((ext_vector_type(2))) __bf16 bf16x2;
#define LAS __attribute__((address_space(3)))
#define GAS __attribute__((address_space(1)))
#define DI __device__ __forceinline__
#define SB0 __builtin_amdgcn_sched_barrier(0)

namespace {
constexpr int L_ = 2048, D_ = 448, N_ = 256, F_ = 1792;
constexpr u32 WS_EPOCH = 0;
constexpr u32 WS_FU = 1024;
constexpr u32 WS_FH = WS_FU + 32 * 128;
constexpr u32 WS_FA = WS_FH + 224 * 128;
constexpr u32 WS_G = 65536;
constexpr u32 WS_UT = 131072;
constexpr u32 WS_AT = WS_UT + D_ * L_ * 2;
constexpr u32 WS_WD = WS_AT + D_ * L_ * 2;
constexpr u32 WS_H = WS_WD + F_ * D_ * 2;
constexpr u32 WS_END = WS_H + L_ * F_ * 2;
constexpr u32 WS_PROF = 16u << 20;
constexpr u32 LD_TAB = 131072;
constexpr u32 LD_KP = 147456;
constexpr u32 KP_STRIDE = 3088;
constexpr u32 LD_SYNC = 163584;
constexpr u32 CP_STRIDE = 35328;
constexpr u32 LD_UP = 70656, UP_STRIDE = 4672;
constexpr u32 LD_GP = 80128;
constexpr u32 SY_PAIR = LD_SYNC, SY_GDONE = LD_SYNC + 16, SY_HCNT = LD_SYNC + 20, SY_ACNT = LD_SYNC + 24,
              SY_CPDONE = LD_SYNC + 28;
constexpr u32 LD_SINK = LD_SYNC - 1024;
constexpr int SPIN_MAX = 1 << 16, SPIN_MAX_LDS = 1 << 20;
#define CODE_PREFETCH_LINES 0
#define KBARMASK ((1u << 0) | (1u << 3) | (1u << 5) | (1u << 8) | (1u << 10) | (1u << 13))

struct Params {
  const u16 *u, *are, *aim, *bre, *bim, *cre, *cim, *ldt, *dsk, *wg, *wu, *wd, *gw;
  u16* out;
  char* ws;
};

DI float bf_lo(u32 x) { return __uint_as_float(x << 16); }
DI float bf_hi(u32 x) { return __uint_as_float(x & 0xffff0000u); }
DI u32 pk_bf16(float a, float b) {
  f32x2 v = {a, b};
  return __builtin_bit_cast(u32, __builtin_convertvector(v, bf16x2));
}
DI u32 pk_bf16(f32x2 v) { return __builtin_bit_cast(u32, __builtin_convertvector(v, bf16x2)); }
DI f32x2 cmul(f32x2 a, f32x2 b) {
  f32x2 t, r;
  asm("v_pk_mul_f32 %0, %1, %2 op_sel_hi:[0,1]" : "=v"(t) : "v"(a), "v"(b));
  asm("v_pk_fma_f32 %0, %1, %2, %3 op_sel:[1,1,0] op_sel_hi:[1,0,1] neg_lo:[0,1,0]" : "=v"(r) : "v"(a), "v"(b), "v"(t));
  return r;
}
DI f32x2 cmul_conj(f32x2 a, f32x2 b) {
  f32x2 t, r;
  asm("v_pk_mul_f32 %0, %1, %2 op_sel_hi:[0,1] neg_hi:[0,1]" : "=v"(t) : "v"(a), "v"(b));
  asm("v_pk_fma_f32 %0, %1, %2, %3 op_sel:[1,1,0] op_sel_hi:[1,0,1] neg_lo:[0,1,0] neg_hi:[0,1,0]"
      : "=v"(r) : "v"(a), "v"(b), "v"(t));
  return r;
}
#define PKM " op_sel_hi:[0,1]\n\t"
#define PKF " op_sel:[1,1,0] op_sel_hi:[1,0,1] neg_lo:[0,1,0]\n\t"
#define PKMC " op_sel_hi:[0,1] neg_hi:[0,1]\n\t"
#define PKFC " op_sel:[1,1,0] op_sel_hi:[1,0,1] neg_lo:[0,1,0] neg_hi:[0,1,0]\n\t"
#define CM4(M, F) \
  asm("v_pk_mul_f32 %0, %4, %8" M "v_pk_mul_f32 %1, %5, %9" M "v_pk_mul_f32 %2, %6, %10" M "v_pk_mul_f32 %3, %7, %11" M \
      "v_pk_fma_f32 %0, %4, %8, %0" F "v_pk_fma_f32 %1, %5, %9, %1" F "v_pk_fma_f32 %2, %6, %10, %2" F "v_pk_fma_f32 %3, %7, %11, %3" F \
      : "=&v"(r[0]), "=&v"(r[1]), "=&v"(r[2]), "=&v"(r[3]) \
      : "v"(a[0]), "v"(a[1]), "v"(a[2]), "v"(a[3]), "v"(b[0]), "v"(b[1]), "v"(b[2]), "v"(b[3]))
DI void cmul4(f32x2 (&r)[4], const f32x2 (&a)[4], const f32x2 (&b)[4]) { CM4(PKM, PKF); }
DI void cmul4_conj(f32x2 (&r)[4], const f32x2 (&a)[4], const f32x2 (&b)[4]) { CM4(PKMC, PKFC); }
DI s16x8 pk4(const f32x2 (&r)[4]) {
  u32x4 o = {pk_bf16(r[0]), pk_bf16(r[1]), pk_bf16(r[2]), pk_bf16(r[3])};
  return __builtin_bit_cast(s16x8, o);
}
DI f32x2 cexp2pi(float x, float y) {
  float e = __builtin_amdgcn_exp2f(x);
  float f = __builtin_amdgcn_fractf(y);
  f32x2 r = {__builtin_amdgcn_cosf(f), __builtin_amdgcn_sinf(f)};
  return r * e;
}
template <int CTRL, int RM> DI float dpp_add(float x) {
  return x + __int_as_float(__builtin_amdgcn_update_dpp(0, __float_as_int(x), CTRL, RM, 0xf, true));
}
DI float wave_sum63(float x) {
  x = dpp_add<0x111, 0xf>(x); x = dpp_add<0x112, 0xf>(x); x = dpp_add<0x114, 0xf>(x); x = dpp_add<0x118, 0xf>(x);
  x = dpp_add<0x142, 0xa>(x); x = dpp_add<0x143, 0xc>(x);
  return x;
}
DI __amdgpu_buffer_rsrc_t mkrs(const void* p, u32 bytes) {
  return __builtin_amdgcn_make_buffer_rsrc((void*)p, 0, bytes, 0x00020000);
}
DI void dma16(u32 ldsaddr, u32 voff, __amdgpu_buffer_rsrc_t rs, u32 soff) {
  asm volatile("s_mov_b32 m0, %0\n\ts_nop 0\n\tbuffer_load_dwordx4 %1, %2, %3 offen lds"
               :: "s"(ldsaddr), "v"(voff), "s"(rs), "s"(soff) : "memory");
}
DI u64 ld_flag(const char* p) { return __hip_atomic_load((const GAS u64*)p, __ATOMIC_RELAXED, __HIP_MEMORY_SCOPE_AGENT); }
DI void st_flag(char* p, u64 v) { __hip_atomic_store((GAS u64*)p, v, __ATOMIC_RELAXED, __HIP_MEMORY_SCOPE_AGENT); }
DI void wait_flags(const char* p, u64 tag) {
#pragma unroll 1
  for (int it = 0; it < SPIN_MAX; ++it) {
    if (__all(ld_flag(p) == tag)) break;
    __builtin_amdgcn_s_sleep(2);
  }
  asm volatile("" ::: "memory");
}
DI void lds_wait_ge(LAS char* lds, u32 off, u32 v) {
#pragma unroll 1
  for (int it = 0; it < SPIN_MAX_LDS; ++it) {
    u32 x = __builtin_amdgcn_readfirstlane(*(volatile LAS u32*)(lds + off));
    if (x >= v) break;
    __builtin_amdgcn_s_sleep(1);
  }
  asm volatile("" ::: "memory");
}
DI void pair_sync(LAS char* lds, int s, u32 epoch) {
  asm volatile("s_waitcnt lgkmcnt(0)" ::: "memory");
  *(volatile LAS u32*)(lds + SY_PAIR + 4 * s) = epoch;
  lds_wait_ge(lds, SY_PAIR + 4 * (s ^ 1), epoch);
}
DI u32 lds_add(LAS char* lds, u32 off, int lane) {
  u32 old = 0;
  if (lane == 0) old = __hip_atomic_fetch_add((LAS u32*)(lds + off), 1u, __ATOMIC_RELAXED, __HIP_MEMORY_SCOPE_WORKGROUP);
  return __builtin_amdgcn_readfirstlane(old);
}
DI s16x8 tr_frag(LAS char* lds, u32 a0, u32 a1) {
  s16x4 x = __builtin_amdgcn_ds_read_tr16_b64_v4i16((LAS s16x4*)(lds + a0));
  s16x4 y = __builtin_amdgcn_ds_read_tr16_b64_v4i16((LAS s16x4*)(lds + a1));
  return __builtin_shufflevector(x, y, 0, 1, 2, 3, 4, 5, 6, 7);
}

#define STAMP(i)

DI void helper_block(const Params& P, LAS char* lds, int s, int tid, int lane, int wave, u64 tag) {
  constexpr u32 RS = 928;
  const int r = tid >> 3, c8 = tid & 7;
  u32x4 xu[7], xw[7], gw[7];
  const u32x4* up = (const u32x4*)(P.u + (size_t)(64 * s + r) * D_) + c8;
#pragma unroll
  for (int i = 0; i < 7; ++i) xu[i] = up[8 * i];
  const bool dow = s < 28;
  const u32x4* wp = (const u32x4*)(P.wd + (size_t)(64 * (dow ? s : 0) + r) * D_) + c8;
#pragma unroll
  for (int i = 0; i < 7; ++i) xw[i] = wp[8 * i];
#pragma unroll
  for (int i = 0; i < 7; ++i) gw[i] = ((const u32x4*)P.gw)[c8 + 8 * i];
  STAMP(2);
  float gs = 0.f;
#pragma unroll
  for (int i = 0; i < 7; ++i) {
    *(LAS u32x4*)(lds + r * RS + 16 * (c8 + 8 * i)) = xu[i];
#pragma unroll
    for (int j = 0; j < 4; ++j) gs += bf_lo(xu[i][j]) * bf_lo(gw[i][j]) + bf_hi(xu[i][j]) * bf_hi(gw[i][j]);
  }
#pragma unroll
  for (int i = 0; i < 7; ++i) *(LAS u32x4*)(lds + 65536 + r * RS + 16 * (c8 + 8 * i)) = xw[i];
  gs += __shfl_xor(gs, 1);
  gs += __shfl_xor(gs, 2);
  gs += __shfl_xor(gs, 4);
  auto rws = mkrs(P.ws, WS_END);
  if (c8 == 0) {
    float g = __builtin_amdgcn_rcpf(1.f + __expf(-gs));
    __builtin_amdgcn_raw_buffer_store_b32(__float_as_uint(g), rws, WS_G + 4 * (64 * s + r), 0, 16);
  }
  STAMP(3);
  __syncthreads();
  STAMP(4);
  const int q = lane >> 4, a = (lane & 15) >> 2, p = lane & 3;
#pragma unroll
  for (int i = 0; i < 7; ++i) {
    int un = wave * 7 + i, dt = un >> 1, th = un & 1;
    u32 ad = (32 * th + 8 * q + a) * RS + 32 * dt + 8 * p;
    s16x8 v = tr_frag(lds, ad, ad + 4 * RS);
    __builtin_amdgcn_raw_buffer_store_b128(__builtin_bit_cast(u32x4, v), rws,
                                           WS_UT + (16 * dt + (lane & 15)) * (L_ * 2) + (64 * s + 32 * th + 8 * q) * 2, 0, 16);
  }
  if (dow) {
#pragma unroll
    for (int i = 0; i < 7; ++i) {
      int un = wave * 7 + i, dt = un >> 1, kh = un & 1;
      u32 ad = 65536 + (32 * kh + 16 * (q >> 1) + 4 * (q & 1) + a) * RS + 32 * dt + 8 * p;
      s16x8 v = tr_frag(lds, ad, ad + 8 * RS);
      __builtin_amdgcn_raw_buffer_store_b128(__builtin_bit_cast(u32x4, v), rws,
                                             WS_WD + (((dt >> 2) * 56 + 2 * s + kh) * 4 + (dt & 3)) * 1024 + lane * 16, 0, 16);
    }
  }
  STAMP(5);
  asm volatile("s_waitcnt vmcnt(0)" ::: "memory");
  STAMP(6);
  __syncthreads();
  if (tid == 0) st_flag(P.ws + WS_FU + 128 * s, tag);
  STAMP(1);
  if (s == 0 && wave == 0) {
    wait_flags(P.ws + WS_FU + 128 * (lane & 31), tag);
#pragma unroll
    for (int i = 0; i < 4; ++i) {
      int b = lane + 64 * i;
      wait_flags(P.ws + WS_FH + 128 * (b < 224 ? b : 0), tag);
    }
    if (lane == 0) st_flag(P.ws + WS_EPOCH, tag);
  }
}

struct G1Frag { s16x8 a[2][2]; s16x8 b[2]; };

template <int C, int KS>
DI void g1_load(G1Frag& f, LAS char* lds, const u32 (&xa)[2][4], const u32 (&wa)[2][2][2]) {
  constexpr u32 sx = ((3 * C) & 7) * 16384u, sw = ((3 * C + 1 + (KS >> 1)) & 7) * 16384u + (KS & 1) * 4096u;
  constexpr u32 hx = sx >> 16, ox = sx & 0xffffu, hw = sw >> 16, ow = sw & 0xffffu;
#pragma unroll
  for (int tt = 0; tt < 2; ++tt) f.b[tt] = *(LAS s16x8*)(lds + xa[hx][KS] + ox + tt * 4096u);
#pragma unroll
  for (int g = 0; g < 2; ++g)
#pragma unroll
    for (int ft = 0; ft < 2; ++ft) f.a[g][ft] = tr_frag(lds, wa[hw][ft][0] + ow + g * 8192u, wa[hw][ft][1] + ow + g * 8192u);
}

struct G1Dma {
  __amdgpu_buffer_rsrc_t rx, rg, ru;
  u32 vx[2], vw[2];
  u32 wofs;
};
template <int U, int I>
DI void g1_dma(const G1Dma& d) {
  if constexpr (U < 21) {
    constexpr int c = U / 3, k = U % 3;
    constexpr u32 slot = (U & 7) * 16384u;
    if constexpr (k == 0) dma16(slot + d.wofs + I * 1024u, d.vx[I & 1], d.rx, (I >> 1) * 16u * 896u + c * 128u);
    else dma16(slot + (I >> 1) * 8192u + (d.wofs >> 1) + (I & 1) * 1024u, d.vw[I & 1], (I >> 1) ? d.ru : d.rg,
               (64u * c + 32u * (k - 1)) * 3584u);
  }
}
template <int U>
DI void g1_dma_unit(const G1Dma& d) { g1_dma<U, 0>(d); g1_dma<U, 1>(d); g1_dma<U, 2>(d); g1_dma<U, 3>(d); }

#define MF32(acc, a, b) acc = __builtin_amdgcn_mfma_f32_32x32x16_bf16(a, b, acc, 0, 0, 0)
#define MF16(acc, a, b) acc = __builtin_amdgcn_mfma_f32_16x16x32_bf16(a, b, acc, 0, 0, 0)

template <int C, int KS, int I>
DI void g1_rd(G1Frag& f, LAS char* lds, const u32 (&xa)[2][4], const u32 (&wa)[2][2][2]) {
  if constexpr (C < 7) {
    constexpr u32 sx = ((3 * C) & 7) * 16384u, sw = ((3 * C + 1 + (KS >> 1)) & 7) * 16384u + (KS & 1) * 4096u;
    constexpr u32 hx = sx >> 16, ox = sx & 0xffffu, hw = sw >> 16, ow = sw & 0xffffu;
    if constexpr (I == 0) {
      f.b[0] = *(LAS s16x8*)(lds + xa[hx][KS] + ox);
      f.b[1] = *(LAS s16x8*)(lds + xa[hx][KS] + ox + 4096u);
    } else {
      constexpr int g = (I - 1) >> 1, ft = (I - 1) & 1;
      f.a[g][ft] = tr_frag(lds, wa[hw][ft][0] + ow + g * 8192u, wa[hw][ft][1] + ow + g * 8192u);
    }
  }
}
template <int X>
DI void g1_piece(const G1Dma& d) {
  if constexpr (X >= 0) g1_dma<X / 4, X % 4>(d);
}
#define G1_PRO 8
constexpr int g1_slot_piece(int C, int KS, int J) {
  const int B = KS == 3 ? C + 1 : C, ph = KS == 3 ? 0 : KS + 1;
  int next = 4 * G1_PRO, res = -1;
  for (int c = 0; c < 7; ++c)
    for (int ks = 0; ks < 4; ++ks)
      for (int j = 0; j < 4; ++j) {
        const int b = ks == 3 ? c + 1 : c;
        const int lim = 4 * (8 + 3 * b);
        int p = -1;
        if (next < lim && next < 84) p = next++;
        if (c == C && ks == KS && j == J) res = p;
      }
  (void)B; (void)ph;
  return res;
}
constexpr int g1_issued_before(int C) {
  int n = 4 * G1_PRO;
  for (int c = 0; c <= C; ++c)
    for (int ks = 0; ks < (c == C ? 3 : 4); ++ks)
      for (int j = 0; j < 4; ++j) n += g1_slot_piece(c, ks, j) >= 0;
  return n;
}
template <int N> DI void wait_vm_barrier() { asm volatile("s_waitcnt vmcnt(%0) lgkmcnt(0)\n\ts_barrier" :: "n"(N) : "memory"); }

template <int C, int KS>
DI void g1_step(f32x16 (&acc)[2][2][2], G1Frag& cur, G1Frag& nxt, LAS char* lds, const u32 (&xa)[2][4],
                const u32 (&wa)[2][2][2], const G1Dma& d) {
  if constexpr (KS == 3 && C < 6) wait_vm_barrier<g1_issued_before(C) - 12 * (C + 2)>();
  constexpr int NC = KS == 3 ? C + 1 : C, NK = KS == 3 ? 0 : KS + 1;
  SB0;
  MF32(acc[0][0][0], cur.a[0][0], cur.b[0]); SB0;
  g1_rd<NC, NK, 0>(nxt, lds, xa, wa); SB0;
  MF32(acc[0][0][1], cur.a[0][0], cur.b[1]); SB0;
  g1_rd<NC, NK, 1>(nxt, lds, xa, wa);
  g1_piece<g1_slot_piece(C, KS, 0)>(d); SB0;
  MF32(acc[0][1][0], cur.a[0][1], cur.b[0]); SB0;
  g1_rd<NC, NK, 2>(nxt, lds, xa, wa); SB0;
  MF32(acc[0][1][1], cur.a[0][1], cur.b[1]); SB0;
  g1_rd<NC, NK, 3>(nxt, lds, xa, wa);
  g1_piece<g1_slot_piece(C, KS, 1)>(d); SB0;
  MF32(acc[1][0][0], cur.a[1][0], cur.b[0]); SB0;
  g1_rd<NC, NK, 4>(nxt, lds, xa, wa); SB0;
  MF32(acc[1][0][1], cur.a[1][0], cur.b[1]); SB0;
  g1_piece<g1_slot_piece(C, KS, 2)>(d); SB0;
  MF32(acc[1][1][0], cur.a[1][1], cur.b[0]); SB0;
  MF32(acc[1][1][1], cur.a[1][1], cur.b[1]); SB0;
  g1_piece<g1_slot_piece(C, KS, 3)>(d); SB0;
}
template <int C>
DI void g1_chunk(f32x16 (&acc)[2][2][2], G1Frag& f0, G1Frag& f1, LAS char* lds, const u32 (&xa)[2][4],
                 const u32 (&wa)[2][2][2], const G1Dma& d) {
  g1_step<C, 0>(acc, f0, f1, lds, xa, wa, d);
  g1_step<C, 1>(acc, f1, f0, lds, xa, wa, d);
  g1_step<C, 2>(acc, f0, f1, lds, xa, wa, d);
  g1_step<C, 3>(acc, f1, f0, lds, xa, wa, d);
}

}

__global__ __launch_bounds__(512, 2) void gated_mix_kernel(Params P) {
  __shared__ __attribute__((aligned(16))) char smem[163840];
  LAS char* lds = (LAS char*)smem;
  const int tid = threadIdx.x, lane = tid & 63;
  const int wave = __builtin_amdgcn_readfirstlane(tid >> 6);
  const int bid = blockIdx.x;
  const u64 rt0 = __builtin_amdgcn_s_memrealtime();
  if (bid < 32) {
    const u64 tag = ld_flag(P.ws + WS_EPOCH) + 1;
    if (bid == 0 && tid == 0) st_flag(P.ws + 512, rt0);
    helper_block(P, lds, bid, tid, lane, wave, tag);
    return;
  }
  const int xcd = bid & 7, slot = (bid >> 3) - 4;
  const int tb = 4 * (xcd >> 1) + slot / 7, fb = 7 * (xcd & 1) + slot % 7;
  const int mid = tb * 14 + fb;
  const int rb = 4 * xcd + slot / 7, cb = slot % 7;
  auto rws = mkrs(P.ws, WS_END);
  s16x8 wdf[7][4];
  u64 tag;

  if (wave < 4) {
    G1Dma d;
    d.rx = mkrs(P.u + (size_t)tb * 128 * D_, 128 * D_ * 2);
    d.rg = mkrs(P.wg + fb * 128, (D_ * F_ - fb * 128) * 2);
    d.ru = mkrs(P.wu + fb * 128, (D_ * F_ - fb * 128) * 2);
    d.wofs = 4096u * wave;
#pragma unroll
    for (int i = 0; i < 2; ++i) {
      d.vx[i] = (32 * wave + 8 * i + (lane >> 3)) * 896 + 16 * ((lane & 7) ^ ((4 * i + (lane >> 4)) & 7));
      d.vw[i] = (8 * wave + 4 * i + (lane >> 4)) * 3584 + 16 * ((lane & 15) ^ (((lane >> 4) << 2) | ((2 * wave + i) & 3)));
    }
    g1_dma_unit<0>(d); g1_dma_unit<1>(d); g1_dma_unit<2>(d);
    if constexpr (G1_PRO > 3) g1_dma_unit<3>(d);
    if constexpr (G1_PRO > 4) g1_dma_unit<4>(d);
    if constexpr (G1_PRO > 5) g1_dma_unit<5>(d);
    if constexpr (G1_PRO > 6) g1_dma_unit<6>(d);
    if constexpr (G1_PRO > 7) g1_dma_unit<7>(d);
    const int wf = wave & 1, wt = wave >> 1, hh = lane >> 5;
    u32 xa[2][4], wa[2][2][2];
#pragma unroll
    for (int ks = 0; ks < 4; ++ks) {
      xa[0][ks] = 128 * (64 * wt + (lane & 31)) + 16 * ((2 * ks + hh) ^ ((lane >> 1) & 7));
      xa[1][ks] = xa[0][ks] + 65536;
      asm volatile("" : "+v"(xa[1][ks]));
    }
    {
      const int blk = (lane >> 4) & 1, q = (lane & 15) >> 2, p = lane & 3;
#pragma unroll
      for (int ft = 0; ft < 2; ++ft)
#pragma unroll
        for (int t = 0; t < 2; ++t) {
          wa[0][ft][t] = 256 * (8 * hh + 4 * t + q) + 16 * ((4 * (2 * wf + ft) + 2 * blk + (p >> 1)) ^ ((q << 2) | ((2 * hh + t) & 3))) + 8 * (p & 1);
          wa[1][ft][t] = wa[0][ft][t] + 65536;
          asm volatile("" : "+v"(wa[1][ft][t]));
        }
    }
    f32x16 acc[2][2][2] = {};
    G1Frag f0, f1;
    wait_vm_barrier<4 * G1_PRO - 12>();
    STAMP(1);
    g1_load<0, 0>(f0, lds, xa, wa);
    SB0;
    g1_chunk<0>(acc, f0, f1, lds, xa, wa, d);
    STAMP(2);
    g1_chunk<1>(acc, f0, f1, lds, xa, wa, d);
    g1_chunk<2>(acc, f0, f1, lds, xa, wa, d);
    g1_chunk<3>(acc, f0, f1, lds, xa, wa, d);
    STAMP(3);
    g1_chunk<4>(acc, f0, f1, lds, xa, wa, d);
    g1_chunk<5>(acc, f0, f1, lds, xa, wa, d);
    g1_chunk<6>(acc, f0, f1, lds, xa, wa, d);
    lds_add(lds, SY_GDONE, lane);
    __builtin_amdgcn_s_setprio(2);
    STAMP(4);
    tag = ld_flag(P.ws + WS_EPOCH) + 1;
    const u64 fu = ld_flag(P.ws + WS_FU + 128 * (lane & 31));
    const u32 hv = WS_H + ((u32)(tb * 8 + 4 * wt + ((lane >> 4) & 1)) * 56 + fb * 4 + 2 * wf) * 1024 + hh * 256 + (lane & 15) * 16;
#pragma unroll
    for (int ft = 0; ft < 2; ++ft)
#pragma unroll
      for (int tt = 0; tt < 2; ++tt)
#pragma unroll
        for (int s = 0; s < 2; ++s) {
          u32x4 o;
#pragma unroll
          for (int j = 0; j < 4; ++j) {
            f32x2 g = {acc[0][ft][tt][8 * s + 2 * j], acc[0][ft][tt][8 * s + 2 * j + 1]};
            f32x2 v = {acc[1][ft][tt][8 * s + 2 * j], acc[1][ft][tt][8 * s + 2 * j + 1]};
            f32x2 e = g * -1.4426950408889634f;
            e.x = __builtin_amdgcn_exp2f(e.x);
            e.y = __builtin_amdgcn_exp2f(e.y);
            e = e + 1.f;
            e.x = __builtin_amdgcn_rcpf(e.x);
            e.y = __builtin_amdgcn_rcpf(e.y);
            o[j] = pk_bf16(g * v * e);
          }
          __builtin_amdgcn_raw_buffer_store_b128(o, rws, hv + (tt * 2 * 56 + ft) * 1024 + s * 512, 0, 16);
        }
    STAMP(5);
    if (!__all(fu == tag)) wait_flags(P.ws + WS_FU + 128 * (lane & 31), tag);
    asm volatile("" ::: "memory");
#pragma unroll
    for (int k = 0; k < 7; ++k)
#pragma unroll
      for (int dt = 0; dt < 4; ++dt)
        wdf[k][dt] = __builtin_bit_cast(s16x8, __builtin_amdgcn_raw_buffer_load_b128(
            rws, lane * 16, WS_WD + ((cb * 56 + 7 * wave + k) * 4 + dt) * 1024, 16));
    asm volatile("s_waitcnt vmcnt(28)" ::: "memory");
    if (lds_add(lds, SY_HCNT, lane) == 3 && lane == 0) st_flag(P.ws + WS_FH + 128 * mid, tag);
    __builtin_amdgcn_s_setprio(0);
    STAMP(6);
  } else {
    __builtin_amdgcn_s_setprio(1);
    const int s = wave - 4, ch = s >> 1, sid = tid - 256, d0 = 2 * mid;
    const int eo = d0 * N_ + 2 * sid;
    const u32 p_are = *(const u32*)(P.are + eo), p_aim = *(const u32*)(P.aim + eo), p_bre = *(const u32*)(P.bre + eo),
              p_bim = *(const u32*)(P.bim + eo), p_cre = *(const u32*)(P.cre + eo), p_cim = *(const u32*)(P.cim + eo);
    const u32 p_ldt = P.ldt[d0 + ch], p_dsk = P.dsk[d0 + ch];
    tag = ld_flag(P.ws + WS_EPOCH) + 1;
    if (sid < 8) *(LAS u32*)(lds + LD_SYNC + 4 * sid) = 0;
    const int tl = lane + 64 * (s & 1);
    u32 kofs[8];
#pragma unroll
    for (int i = 0; i < 16; ++i) {
      const u32 l = 2047 - 8 * (tl + 128 * (i >> 3)) - (i & 7);
      const u32 v = (u32)bf_lo(pk_bf16((float)l, 0.f));
      const u32 o = (v & ~3u) + 16 * ((0xb1918100u >> (8 * (v & 3))) & 255u);
      if (i & 1) kofs[i >> 1] |= o << 16; else kofs[i >> 1] = o;
    }
    SB0;
    asm volatile("s_waitcnt lgkmcnt(0)\n\ts_barrier" ::: "memory");
    SB0;
    STAMP(1);
    LAS char* kp = lds + LD_KP + KP_STRIDE * s;
    {
      const float dt = bf_lo(pk_bf16(expf(bf_lo(p_ldt)), 0.f));
      float k2 = 0.f;
#pragma unroll
      for (int i = 0; i < 2; ++i) {
        const float ar = i ? bf_hi(p_are) : bf_lo(p_are), ai = i ? bf_hi(p_aim) : bf_lo(p_aim);
        const f32x2 B = {dt * (i ? bf_hi(p_bre) : bf_lo(p_bre)), dt * (i ? bf_hi(p_bim) : bf_lo(p_bim))};
        const f32x2 Cc = {i ? bf_hi(p_cre) : bf_lo(p_cre), i ? bf_hi(p_cim) : bf_lo(p_cim)};
        const float a2 = (dt * ar) * 1.4426950408889634f, b2 = (dt * ai) * 0.15915494309189535f;
        const f32x2 W = cmul(Cc, B);
        const f32x2 z1 = cexp2pi(a2, b2), zk = cexp2pi(1024.f * a2, 1024.f * b2);
        k2 += cmul(W, cmul(zk, zk)).x;
        f32x4 t0 = {a2, b2, W.x, W.y}, t1 = {z1.x, z1.y, zk.x, zk.y};
        *(LAS f32x4*)(lds + LD_TAB + 64 * sid + 32 * i) = t0;
        *(LAS f32x4*)(lds + LD_TAB + 64 * sid + 32 * i + 16) = t1;
      }
      k2 = wave_sum63(k2);
      if (lane == 63) *(LAS float*)(kp + 4 * 512) = k2;
      asm volatile("" :: "v"(tag));
    }
    STAMP(2);
    {
      f32x4 ka[5] = {};
      const float pw = 4.f * (lane & 15);
      u32 tab = LD_TAB + 4096 * s + 128 * (lane >> 4);
      asm volatile("" : "+v"(tab));
      f32x4 t0[4], t1[4];
#pragma unroll
      for (int i = 0; i < 4; ++i) t0[i] = *(LAS f32x4*)(lds + tab + 32 * i);
#pragma unroll 1
      for (int ks = 0; ks < 8; ++ks) {
        f32x2 z[4], w[4], y[4], c[4];
#pragma unroll
        for (int i = 0; i < 4; ++i) t1[i] = *(LAS f32x4*)(lds + tab + 32 * i + 16);
#pragma unroll
        for (int i = 0; i < 4; ++i) {
          const f32x2 xy = t0[i].xy * pw;
          z[i] = cexp2pi(xy.x, xy.y);
          c[i] = t0[i].zw;
        }
        cmul4_conj(w, z, c);
        tab += 512;
#pragma unroll
        for (int i = 0; i < 4; ++i) t0[i] = *(LAS f32x4*)(lds + tab + 32 * i);
        const s16x8 a = pk4(w);
        if ((KBARMASK >> (2 * ks)) & 1) asm volatile("s_barrier" ::: "memory");
        cmul4(y, z, z); cmul4(z, y, y); cmul4(y, z, z); cmul4(z, y, y);
        MF16(ka[0], a, pk4(z));
#pragma unroll
        for (int i = 0; i < 4; ++i) c[i] = t1[i].zw;
        cmul4(y, z, c);
        MF16(ka[4], a, pk4(y));
#pragma unroll
        for (int i = 0; i < 4; ++i) c[i] = t1[i].xy;
        cmul4(y, z, c);
        MF16(ka[1], a, pk4(y));
        cmul4(z, y, c);
        MF16(ka[2], a, pk4(z));
        cmul4(y, z, c);
        MF16(ka[3], a, pk4(y));
        if ((KBARMASK >> (2 * ks + 1)) & 1) asm volatile("s_barrier" ::: "memory");
      }
      const u32 o = 64 * (lane & 15) + 16 * (lane >> 4);
      *(LAS f32x4*)(kp + o) = ka[0];
      *(LAS f32x4*)(kp + 1024 + o) = ka[4];
      if ((lane & 15) < 8) *(LAS f32x4*)(kp + 4 * 580 + o) = ka[2];
      if ((lane & 15) < 4) {
        *(LAS f32x4*)(kp + 4 * 516 + o) = ka[1];
        *(LAS f32x4*)(kp + 4 * 708 + o) = ka[3];
      }
    }
    STAMP(3);
    wait_flags(P.ws + WS_FU + 128 * (lane & 31), tag);
    u32x4 urow[2];
#pragma unroll
    for (int i = 0; i < 2; ++i)
      urow[i] = __builtin_amdgcn_raw_buffer_load_b128(rws, 16 * tl, WS_UT + (d0 + ch) * (L_ * 2) + 2048 * i, 16);
#pragma unroll
    for (int k = 0; k < 7; ++k)
#pragma unroll
      for (int dt = 0; dt < 4; ++dt)
        wdf[k][dt] = __builtin_bit_cast(s16x8, __builtin_amdgcn_raw_buffer_load_b128(
            rws, lane * 16, WS_WD + ((cb * 56 + 7 * wave + k) * 4 + dt) * 1024, 16));
    pair_sync(lds, s, 1);
    LAS char* rk = lds + LD_TAB + 8192 * ch;
    {
      LAS char* k0 = lds + LD_KP + KP_STRIDE * 2 * ch;
#pragma unroll
      for (int b = 0; b < 2; ++b) {
        u32x4 o;
#pragma unroll
        for (int j = 0; j < 4; ++j) {
          const u32 w = kofs[4 * b + j], lo = w & 0xffffu, hi = w >> 16;
          o[j] = pk_bf16(*(LAS float*)(k0 + lo) + *(LAS float*)(k0 + KP_STRIDE + lo),
                         *(LAS float*)(k0 + hi) + *(LAS float*)(k0 + KP_STRIDE + hi));
        }
        *(LAS u32x4*)(rk + 16 * (tl + 128 * b)) = o;
      }
      if (tl == 0) *(LAS u32x4*)(rk + 4096) = u32x4{0, 0, 0, 0};
    }
    pair_sync(lds, s, 2);
    STAMP(4);
    lds_wait_ge(lds, SY_GDONE, 4);
    STAMP(5);
    LAS char* cp = lds + CP_STRIDE * ch;
    LAS char* upl = lds + LD_UP + UP_STRIDE * ch;
    {
      const u32x4 z4 = {0, 0, 0, 0};
      *(LAS u32x4*)(cp + 16 * tl) = z4;
      if (tl < 24) *(LAS u32x4*)(cp + 2048 + 16 * tl) = z4;
      else if (tl < 32) *(LAS u32x4*)(cp + 275 * 128 + 16 * (tl - 24)) = z4;
      else if (tl < 64) *(LAS u32x4*)(upl + 16 * (tl - 32)) = z4;
      else if (tl < 68) *(LAS u32x4*)(upl + 4608 + 16 * (tl - 64)) = z4;
#pragma unroll
      for (int b = 0; b < 2; ++b) {
        const int be = tl + 128 * b;
        const u32x4 lo = *(LAS u32x4*)(rk + 16 * (255 - be)), hi = *(LAS u32x4*)(rk + 16 * (256 - be));
        const u32 dd[8] = {lo[0], lo[1], lo[2], lo[3], hi[0], hi[1], hi[2], hi[3]};
        u32 aa[7];
#pragma unroll
        for (int k = 0; k < 7; ++k) aa[k] = __builtin_amdgcn_alignbit(dd[k + 1], dd[k], 16);
        const u32 base = 128 * (be + 19) + 16 * ((be + 19) & 7);
#pragma unroll
        for (int sh = 0; sh < 8; ++sh) {
          const int o = (7 - sh) >> 1;
          const u32x4 v = (sh & 1) ? u32x4{dd[o], dd[o + 1], dd[o + 2], dd[o + 3]} : u32x4{aa[o], aa[o + 1], aa[o + 2], aa[o + 3]};
          *(LAS u32x4*)(cp + (base ^ (16 * sh))) = v;
        }
      }
#pragma unroll
      for (int i = 0; i < 2; ++i) *(LAS u32x4*)(upl + 512 + 2048 * i + 16 * tl) = urow[i];
    }
    pair_sync(lds, s, 3);
    lds_add(lds, SY_CPDONE, lane);
    STAMP(6);
    f32x4 ya[8] = {};
    {
      const int c = lane & 7, hi = (lane >> 3) & 1, q = lane >> 4;
      const u32 ub = 512 + 16 * ((lane & 15) + q);
#pragma unroll
      for (int i = 0; i < 4; ++i) {
        const bool up = (s & 1);
        const int rho = i < 3 ? (up ? 5 + i : i) : (up ? 3 : 4);
        const bool sft = i < 3 && up;
        const int bq = 16 * hi - q + 19 - 4 * rho + (sft ? 32 : 0);
        const u32 ab = 128 * bq + 16 * (c ^ (bq & 7));
        const u32 bb = ub + 64 * rho - (sft ? 512 : 0);
        s16x8 bf[8];
#pragma unroll
        for (int p = 0; p < 8; ++p) bf[p] = *(LAS s16x8*)(upl + bb + 512 * p);
#pragma unroll
        for (int e = 0; e < 8; ++e) {
          const s16x8 af = *(LAS s16x8*)(cp + ab + 4096 * e);
#pragma unroll
          for (int jt = e; jt < 8; ++jt) MF16(ya[jt], af, bf[jt - e]);
        }
        if (i == 3 && !up) {
          const s16x8 bm = *(LAS s16x8*)(upl + bb - 512);
#pragma unroll
          for (int jt = 0; jt < 8; ++jt) {
            const s16x8 af = *(LAS s16x8*)(cp + ab + 4096 * (jt + 1));
            MF16(ya[jt], af, bm);
          }
        }
      }
    }
    STAMP(7);
    pair_sync(lds, s, 4);
    {
      const bool up = (s & 1);
      f32x4 keep[4];
#pragma unroll
      for (int j = 0; j < 4; ++j) {
        f32x4 lo = ya[j], hi = ya[4 + j];
        asm volatile("" : "+v"(lo), "+v"(hi));
        keep[j] = up ? hi : lo;
        *(LAS f32x4*)(cp + 32768 - 8192 + 4096 * (s & 1) + 1024 * j + 16 * lane) = up ? lo : hi;
      }
      pair_sync(lds, s, 5);
      const float dsk = bf_lo(p_dsk);
      const int q = lane >> 4;
      const u32 tq = 128 * (q >> 1) + 8 * (lane & 15) + 4 * (q & 1) + (up ? 1024 : 0);
#pragma unroll
      for (int j = 0; j < 4; ++j) {
        f32x4 y = *(LAS f32x4*)(cp + 32768 - 8192 + 4096 * ((s & 1) ^ 1) + 1024 * j + 16 * lane) + keep[j];
        const u32x2 uu = *(LAS u32x2*)(upl + 512 + 2 * (tq + 256 * j));
        u32x2 o = {pk_bf16(y[0] + dsk * bf_lo(uu[0]), y[1] + dsk * bf_hi(uu[0])),
                   pk_bf16(y[2] + dsk * bf_lo(uu[1]), y[3] + dsk * bf_hi(uu[1]))};
        __builtin_amdgcn_raw_buffer_store_b64(o, rws, 2 * tq, WS_AT + (d0 + ch) * (L_ * 2) + 512 * j, 16);
      }
      pair_sync(lds, s, 6);
    }
    asm volatile("s_waitcnt vmcnt(0)" ::: "memory");
    __builtin_amdgcn_s_waitcnt(0x0F70);
    if (lds_add(lds, SY_ACNT, lane) == 3 && lane == 0) st_flag(P.ws + WS_FA + 128 * mid, tag);
    __builtin_amdgcn_s_setprio(0);
    STAMP(8);
  }

  {
    const int j0 = (7 * wave) >> 2, j1 = (7 * wave + 6) >> 2;
    const int jj = j0 + (lane & 3);
    wait_flags(P.ws + WS_FH + 128 * ((rb >> 1) * 14 + (jj < j1 ? jj : j1)), tag);
    STAMP(9);
    s16x8 hf[4][7];
    const u32 hs = WS_H + ((u32)rb * 4 * 56 + 7 * wave) * 1024;
#pragma unroll
    for (int tt = 0; tt < 3; ++tt)
#pragma unroll
      for (int k = 0; k < 7; ++k)
        hf[tt][k] = __builtin_bit_cast(s16x8, __builtin_amdgcn_raw_buffer_load_b128(rws, lane * 16, hs + (tt * 56 + k) * 1024, 16));
    const u32 pb = wave < 3 ? LD_GP + 16384 * wave : wave == 3 ? LD_TAB : CP_STRIDE * ((wave - 4) >> 1) + 16384 * (wave & 1);
    if (wave == 3) lds_wait_ge(lds, SY_CPDONE, 4);
    LAS char* pp = lds + pb + 256 * (lane & 15);
    SB0;
#pragma unroll
    for (int tt = 0; tt < 4; ++tt) {
      f32x4 oa[4] = {};
#pragma unroll
      for (int k = 0; k < 7; ++k)
#pragma unroll
        for (int dt = 0; dt < 4; ++dt) MF16(oa[dt], wdf[k][dt], hf[tt][k]);
      if (tt == 0) {
        SB0;
#pragma unroll
        for (int k = 0; k < 7; ++k)
          hf[3][k] = __builtin_bit_cast(s16x8, __builtin_amdgcn_raw_buffer_load_b128(rws, lane * 16, hs + (3 * 56 + k) * 1024, 16));
        SB0;
      }
#pragma unroll
      for (int dt = 0; dt < 4; ++dt) *(LAS f32x4*)(pp + 4096 * tt + 16 * ((4 * dt + (lane >> 4)) ^ (lane & 15))) = oa[dt];
    }
    STAMP(10);
    const int t = 64 * rb + 16 * (wave >> 1) + (lane & 15);
    const float g = __uint_as_float(__builtin_amdgcn_raw_buffer_load_b32(rws, 4 * t, WS_G, 16));
    if (wave < 4) {
      wait_flags(P.ws + WS_FA + 128 * (32 * cb + (lane & 31)), tag);
      lds_wait_ge(lds, SY_CPDONE, 4);
      u32x4 av[2];
#pragma unroll
      for (int i = 0; i < 2; ++i)
        av[i] = __builtin_amdgcn_raw_buffer_load_b128(rws, (u32)(64 * cb + (tid >> 3) + 32 * i) * (L_ * 2) + 16 * (tid & 7), WS_AT + 128 * rb, 16);
#pragma unroll
      for (int i = 0; i < 2; ++i) *(LAS u32x4*)(lds + LD_KP + 144 * ((tid >> 3) + 32 * i) + 16 * (tid & 7)) = av[i];
    }
    STAMP(11);
    asm volatile("s_waitcnt lgkmcnt(0)\n\ts_barrier" ::: "memory");
    STAMP(12);
    const int gi = lane >> 4, dh = wave & 1;
    f32x4 b0 = {}, b1 = {};
    const u32 ro = 4096 * (wave >> 1) + 256 * (lane & 15);
    const u32 r0 = ro + 16 * ((8 * dh + 2 * gi) ^ (lane & 15)), r1 = ro + 16 * ((8 * dh + 2 * gi + 1) ^ (lane & 15));
#pragma unroll
    for (int w = 0; w < 8; ++w) {
      const u32 o = w < 3 ? LD_GP + 16384 * w : w == 3 ? LD_TAB : CP_STRIDE * ((w - 4) >> 1) + 16384 * (w & 1);
      b0 += *(LAS f32x4*)(lds + o + r0);
      b1 += *(LAS f32x4*)(lds + o + r1);
    }
    const u32 aad = LD_KP + 144 * (32 * dh + 8 * gi + ((lane & 15) >> 2)) + 32 * (wave >> 1) + 8 * (lane & 3);
    const u32x4 av = __builtin_bit_cast(u32x4, tr_frag(lds, aad, aad + 4 * 144));
    const float og = 1.f - g;
    u32x4 o;
    o[0] = pk_bf16(g * bf_lo(av[0]) + og * b0[0], g * bf_hi(av[0]) + og * b0[1]);
    o[1] = pk_bf16(g * bf_lo(av[1]) + og * b0[2], g * bf_hi(av[1]) + og * b0[3]);
    o[2] = pk_bf16(g * bf_lo(av[2]) + og * b1[0], g * bf_hi(av[2]) + og * b1[1]);
    o[3] = pk_bf16(g * bf_lo(av[3]) + og * b1[2], g * bf_hi(av[3]) + og * b1[3]);
    *(u32x4*)(P.out + (size_t)t * D_ + 64 * cb + 32 * dh + 8 * gi) = o;
    const u64 g0 = ld_flag(P.ws + 512);
    for (int it = 0; it < 1000000 && (long long)(__builtin_amdgcn_s_memrealtime() - g0) < 4000; ++it) __builtin_amdgcn_s_sleep(1);
  }
}

extern "C" void kernel_launch(void* const* d_in, const int* in_sizes, int n_in, void* d_out, int out_size, void* d_ws,
                              size_t ws_size, hipStream_t stream) {
  Params P;
  P.u = (const u16*)d_in[0];
  P.are = (const u16*)d_in[1];
  P.aim = (const u16*)d_in[2];
  P.bre = (const u16*)d_in[3];
  P.bim = (const u16*)d_in[4];
  P.cre = (const u16*)d_in[5];
  P.cim = (const u16*)d_in[6];
  P.ldt = (const u16*)d_in[7];
  P.dsk = (const u16*)d_in[8];
  P.wg = (const u16*)d_in[10];
  P.wu = (const u16*)d_in[11];
  P.wd = (const u16*)d_in[12];
  P.gw = (const u16*)d_in[13];
  P.out = (u16*)d_out;
  P.ws = (char*)d_ws;
  gated_mix_kernel<<<256, 512, 0, stream>>>(P);
}
```

```cpp
#include <hip/hip_runtime.h>

typedef unsigned short u16;
typedef unsigned int u32;
typedef unsigned long long u64;
typedef __attribute__((ext_vector_type(8))) short s16x8;
typedef __attribute__((ext_vector_type(4))) short s16x4;
typedef __attribute__((ext_vector_type(2))) float f32x2;
typedef __attribute__((ext_vector_type(4))) float f32x4;
typedef __attribute__((ext_vector_type(16))) float f32x16;
typedef __attribute__((ext_vector_type(2))) u32 u32x2;
typedef __attribute__((ext_vector_type(4))) u32 u32x4;
typedef __attribute__((ext_vector_type(2))) __bf16 bf16x2;
#define LAS __attribute__((address_space(3)))
#define GAS __attribute__((address_space(1)))
#define DI __device__ __forceinline__
#define SB0 __builtin_amdgcn_sched_barrier(0)
#define SBV __builtin_amdgcn_sched_barrier(0x8 | 0x4 | 0x10 | 0x80)

namespace {
constexpr int L_ = 2048, D_ = 448, N_ = 256, F_ = 1792;
constexpr u32 WS_EPOCH = 0;
constexpr u32 WS_FW = 1024;
constexpr u32 WS_FU = WS_FW + 32 * 128;
constexpr u32 WS_FG = WS_FU + 32 * 128;
constexpr u32 WS_FH = WS_FG + 32 * 128;
constexpr u32 WS_G = 98304;
constexpr u32 WS_UT = 131072;
constexpr u32 TS = L_ * 2 + 128;
constexpr u32 AS = L_ * 8 + 128;
constexpr u32 WS_AT = WS_UT + D_ * TS;
constexpr u32 WS_WD = WS_AT + 224 * AS;
constexpr u32 WS_H = WS_WD + F_ * D_ * 2;
constexpr u32 WS_END = WS_H + L_ * F_ * 2;
constexpr u32 WS_PROF = 16u << 20;
constexpr u32 LD_TB = 98304, TB_STRIDE = 5184;
constexpr u32 LD_KR = 155648, KR_STRIDE = 3584;
constexpr u32 LD_KP = 49152;
constexpr u32 LD_RK = 147456;
constexpr u32 LD_CP = 49152, CP_STRIDE = 35328;
constexpr u32 LD_UP = 119808, UP_STRIDE = 4672;
constexpr u32 LD_EX = 0, LD_EXO = 131072;
constexpr u32 LD_AT = 147456;
constexpr int SPIN_MAX = 1 << 16;
#define CODE_PREFETCH_LINES 0
struct Params {
  const u16 *u, *are, *aim, *bre, *bim, *cre, *cim, *ldt, *dsk, *wg, *wu, *wd, *gw;
  u16* out;
  char* ws;
};

DI float bf_lo(u32 x) { return __uint_as_float(x << 16); }
DI float bf_hi(u32 x) { return __uint_as_float(x & 0xffff0000u); }
DI u32 pk_bf16(float a, float b) {
  f32x2 v = {a, b};
  return __builtin_bit_cast(u32, __builtin_convertvector(v, bf16x2));
}
DI u32 pk_bf16(f32x2 v) { return __builtin_bit_cast(u32, __builtin_convertvector(v, bf16x2)); }
struct cf { float x, y; };
DI cf cmul(cf a, cf b) { return {__builtin_fmaf(a.x, b.x, -(a.y * b.y)), __builtin_fmaf(a.x, b.y, a.y * b.x)}; }
DI cf cexp2pi(float x, float y) {
  const float e = __builtin_amdgcn_exp2f(x), f = __builtin_amdgcn_fractf(y);
  return {e * __builtin_amdgcn_cosf(f), e * __builtin_amdgcn_sinf(f)};
}
typedef __attribute__((ext_vector_type(2))) _Float16 h2;
typedef __attribute__((ext_vector_type(8))) _Float16 h8;
DI h2 pkh(cf a) { f32x2 v = {a.x, a.y}; return __builtin_convertvector(v, h2); }
#define PKM " op_sel_hi:[0,1]\n\t"
#define PKF " op_sel:[1,1,0] op_sel_hi:[1,0,1] neg_lo:[0,1,0]\n\t"
DI u32x4 cmulh4(u32x4 a, u32x4 b) {
  u32 r0, r1, r2, r3;
  const u32 a0 = a[0], a1 = a[1], a2 = a[2], a3 = a[3], b0 = b[0], b1 = b[1], b2 = b[2], b3 = b[3];
  asm("v_pk_mul_f16 %0, %4, %8" PKM "v_pk_mul_f16 %1, %5, %9" PKM "v_pk_mul_f16 %2, %6, %10" PKM "v_pk_mul_f16 %3, %7, %11" PKM
      "v_pk_fma_f16 %0, %4, %8, %0" PKF "v_pk_fma_f16 %1, %5, %9, %1" PKF "v_pk_fma_f16 %2, %6, %10, %2" PKF
      "v_pk_fma_f16 %3, %7, %11, %3 op_sel:[1,1,0] op_sel_hi:[1,0,1] neg_lo:[0,1,0]"
      : "=&v"(r0), "=&v"(r1), "=&v"(r2), "=&v"(r3)
      : "v"(a0), "v"(a1), "v"(a2), "v"(a3), "v"(b0), "v"(b1), "v"(b2), "v"(b3));
  return u32x4{r0, r1, r2, r3};
}
DI u32x4 mulh4(u32x4 a, u32x4 b) {
  u32 r0, r1, r2, r3;
  const u32 a0 = a[0], a1 = a[1], a2 = a[2], a3 = a[3], b0 = b[0], b1 = b[1], b2 = b[2], b3 = b[3];
  asm("v_pk_mul_f16 %0, %4, %8" PKM "v_pk_mul_f16 %1, %5, %9" PKM "v_pk_mul_f16 %2, %6, %10" PKM "v_pk_mul_f16 %3, %7, %11 op_sel_hi:[0,1]"
      : "=&v"(r0), "=&v"(r1), "=&v"(r2), "=&v"(r3)
      : "v"(a0), "v"(a1), "v"(a2), "v"(a3), "v"(b0), "v"(b1), "v"(b2), "v"(b3));
  return u32x4{r0, r1, r2, r3};
}
DI u32x4 fmah4(u32x4 r, u32x4 a, u32x4 b) {
  u32 r0 = r[0], r1 = r[1], r2 = r[2], r3 = r[3];
  const u32 a0 = a[0], a1 = a[1], a2 = a[2], a3 = a[3], b0 = b[0], b1 = b[1], b2 = b[2], b3 = b[3];
  asm("v_pk_fma_f16 %0, %4, %8, %0" PKF "v_pk_fma_f16 %1, %5, %9, %1" PKF "v_pk_fma_f16 %2, %6, %10, %2" PKF
      "v_pk_fma_f16 %3, %7, %11, %3 op_sel:[1,1,0] op_sel_hi:[1,0,1] neg_lo:[0,1,0]"
      : "+v"(r0), "+v"(r1), "+v"(r2), "+v"(r3)
      : "v"(a0), "v"(a1), "v"(a2), "v"(a3), "v"(b0), "v"(b1), "v"(b2), "v"(b3));
  return u32x4{r0, r1, r2, r3};
}
DI void cmul4(f32x2 (&r)[4], const f32x2 (&a)[4], const f32x2 (&b)[4]) {
  asm("v_pk_mul_f32 %0, %4, %8" PKM "v_pk_mul_f32 %1, %5, %9" PKM "v_pk_mul_f32 %2, %6, %10" PKM "v_pk_mul_f32 %3, %7, %11" PKM
      "v_pk_fma_f32 %0, %4, %8, %0" PKF "v_pk_fma_f32 %1, %5, %9, %1" PKF "v_pk_fma_f32 %2, %6, %10, %2" PKF
      "v_pk_fma_f32 %3, %7, %11, %3 op_sel:[1,1,0] op_sel_hi:[1,0,1] neg_lo:[0,1,0]"
      : "=&v"(r[0]), "=&v"(r[1]), "=&v"(r[2]), "=&v"(r[3])
      : "v"(a[0]), "v"(a[1]), "v"(a[2]), "v"(a[3]), "v"(b[0]), "v"(b[1]), "v"(b[2]), "v"(b[3]));
}
DI u32 cvth(f32x2 v) { return __builtin_bit_cast(u32, __builtin_convertvector(v, h2)); }
DI u32 cvth_conj(f32x2 v) {
  u32 r;
  const float x = v.x, y = v.y;
  asm("v_cvt_pk_f16_f32 %0, %1, -%2" : "=v"(r) : "v"(x), "v"(y));
  return r;
}
template <int CTRL, int RM> DI u32 dpp_umax(u32 x) {
  const u32 y = __builtin_amdgcn_update_dpp(0u, x, CTRL, RM, 0xf, true);
  return x > y ? x : y;
}
#define H8(v) __builtin_bit_cast(h8, v)
template <int CTRL, int RM> DI float dpp_max(float x) {
  return __builtin_fmaxf(x, __int_as_float(__builtin_amdgcn_update_dpp(0, __float_as_int(x), CTRL, RM, 0xf, true)));
}
template <int CTRL, int RM> DI float dpp_add(float x) {
  return x + __int_as_float(__builtin_amdgcn_update_dpp(0, __float_as_int(x), CTRL, RM, 0xf, true));
}
DI float wave_sum63(float x) {
  x = dpp_add<0x111, 0xf>(x); x = dpp_add<0x112, 0xf>(x); x = dpp_add<0x114, 0xf>(x); x = dpp_add<0x118, 0xf>(x);
  x = dpp_add<0x142, 0xa>(x); x = dpp_add<0x143, 0xc>(x);
  return x;
}
DI float wave_max(float x) {
  x = dpp_max<0x111, 0xf>(x); x = dpp_max<0x112, 0xf>(x); x = dpp_max<0x114, 0xf>(x); x = dpp_max<0x118, 0xf>(x);
  x = dpp_max<0x142, 0xa>(x); x = dpp_max<0x143, 0xc>(x);
  return __int_as_float(__builtin_amdgcn_readlane(__float_as_int(x), 63));
}
DI void pk4_mul_s(f32x2 (&r)[4], const f32x2 (&a)[4], f32x2 c) {
  asm("v_pk_mul_f32 %0, %4, %8\n\tv_pk_mul_f32 %1, %5, %8\n\tv_pk_mul_f32 %2, %6, %8\n\tv_pk_mul_f32 %3, %7, %8"
      : "=&v"(r[0]), "=&v"(r[1]), "=&v"(r[2]), "=&v"(r[3]) : "v"(a[0]), "v"(a[1]), "v"(a[2]), "v"(a[3]), "s"(c));
}
DI void pk4_mul(f32x2 (&r)[4], const f32x2 (&a)[4], const f32x2 (&b)[4]) {
  asm("v_pk_mul_f32 %0, %4, %8\n\tv_pk_mul_f32 %1, %5, %9\n\tv_pk_mul_f32 %2, %6, %10\n\tv_pk_mul_f32 %3, %7, %11"
      : "=&v"(r[0]), "=&v"(r[1]), "=&v"(r[2]), "=&v"(r[3])
      : "v"(a[0]), "v"(a[1]), "v"(a[2]), "v"(a[3]), "v"(b[0]), "v"(b[1]), "v"(b[2]), "v"(b[3]));
}
DI void pk4_add1(f32x2 (&r)[4]) {
  asm("s_nop 0\n\tv_pk_add_f32 %0, %0, 1.0 op_sel_hi:[1,0]\n\tv_pk_add_f32 %1, %1, 1.0 op_sel_hi:[1,0]\n\t"
      "v_pk_add_f32 %2, %2, 1.0 op_sel_hi:[1,0]\n\tv_pk_add_f32 %3, %3, 1.0 op_sel_hi:[1,0]"
      : "+v"(r[0]), "+v"(r[1]), "+v"(r[2]), "+v"(r[3]));
}
DI f32x4 add4(f32x4 a, f32x4 b) {
  float r0, r1, r2, r3;
  const float a0 = a[0], a1 = a[1], a2 = a[2], a3 = a[3], b0 = b[0], b1 = b[1], b2 = b[2], b3 = b[3];
  asm("v_add_f32 %0, %4, %8\n\tv_add_f32 %1, %5, %9\n\tv_add_f32 %2, %6, %10\n\tv_add_f32 %3, %7, %11"
      : "=&v"(r0), "=&v"(r1), "=&v"(r2), "=&v"(r3) : "v"(a0), "v"(a1), "v"(a2), "v"(a3), "v"(b0), "v"(b1), "v"(b2), "v"(b3));
  return f32x4{r0, r1, r2, r3};
}
DI f32x4 mul4(f32x4 a, float b) {
  float r0, r1, r2, r3;
  const float a0 = a[0], a1 = a[1], a2 = a[2], a3 = a[3];
  asm("v_mul_f32 %0, %8, %4\n\tv_mul_f32 %1, %8, %5\n\tv_mul_f32 %2, %8, %6\n\tv_mul_f32 %3, %8, %7"
      : "=&v"(r0), "=&v"(r1), "=&v"(r2), "=&v"(r3) : "v"(a0), "v"(a1), "v"(a2), "v"(a3), "s"(b));
  return f32x4{r0, r1, r2, r3};
}
DI bool all_true(bool p) { return __all(p); }
DI bool all_eq(u64 a, u64 b) { return __all(a == b); }
DI float dexpf(float x) { return expf(x); }
DI float rbf(float x) { return bf_lo(pk_bf16(x, 0.f)); }
DI __amdgpu_buffer_rsrc_t mkrs(const void* p, u32 bytes) {
  return __builtin_amdgcn_make_buffer_rsrc((void*)p, 0, bytes, 0x00020000);
}
DI void dma16(u32 ldsaddr, u32 voff, __amdgpu_buffer_rsrc_t rs, u32 soff) {
  asm volatile("s_mov_b32 m0, %0\n\ts_nop 0\n\tbuffer_load_dwordx4 %1, %2, %3 offen lds"
               :: "s"(ldsaddr), "v"(voff), "s"(rs), "s"(soff) : "memory");
}
DI void gdma4(u32 ldsaddr, u32 voff, const void* base) {
  asm volatile("s_mov_b32 m0, %0\n\ts_nop 0\n\tglobal_load_lds_dword %1, %2" :: "s"(ldsaddr), "v"(voff), "s"(base) : "memory");
}
DI void gdma16(u32 ldsaddr, u32 voff, const void* base) {
  asm volatile("s_mov_b32 m0, %0\n\ts_nop 0\n\tglobal_load_lds_dwordx4 %1, %2" :: "s"(ldsaddr), "v"(voff), "s"(base) : "memory");
}
#define CAS __attribute__((address_space(4)))
DI u64 ld_flag(const char* p) { return __hip_atomic_load((const GAS u64*)p, __ATOMIC_RELAXED, __HIP_MEMORY_SCOPE_AGENT); }
DI void st_flag(char* p, u64 v) { __hip_atomic_store((GAS u64*)p, v, __ATOMIC_RELAXED, __HIP_MEMORY_SCOPE_AGENT); }
DI void wait_flags(const char* p, u64 tag) {
#pragma unroll 1
  for (int it = 0; it < SPIN_MAX; ++it) {
    if (__all(ld_flag(p) == tag)) break;
    __builtin_amdgcn_s_sleep(2);
  }
  asm volatile("" ::: "memory");
}
template <int N> DI void publish(__amdgpu_buffer_rsrc_t rs, u32 off, u64 tagv) {
  asm volatile("s_waitcnt vmcnt(%3)\n\ts_mov_b64 exec, 1\n\tbuffer_store_dwordx2 %0, off, %1, %2 sc1\n\ts_mov_b64 exec, -1"
               :: "v"(tagv), "s"(rs), "s"(off), "n"(N) : "memory");
}
DI s16x8 tr_frag(LAS char* lds, u32 a0, u32 a1) {
  s16x4 x = __builtin_amdgcn_ds_read_tr16_b64_v4i16((LAS s16x4*)(lds + a0));
  s16x4 y = __builtin_amdgcn_ds_read_tr16_b64_v4i16((LAS s16x4*)(lds + a1));
  return __builtin_shufflevector(x, y, 0, 1, 2, 3, 4, 5, 6, 7);
}

#define STAMP(i)
#define STAMP_DECL
#define FALLBACK(b)
#define PROF_ARGS
#define PROF_PASS
#define STAMP_OUT

DI void helper_block(const Params& P, LAS char* lds, int s, int tid, int lane, int wave) {
  STAMP_DECL;
  STAMP(0);
  constexpr u32 RS = 928;
  const int r = tid >> 3, c8 = tid & 7;
  const bool dow = s < 28;
  u32x4 xw[7], xu[7], gw[7];
  const u32x4* wp = (const u32x4*)(P.wd + (size_t)(64 * (dow ? s : 0) + r) * D_) + c8;
#pragma unroll
  for (int i = 0; i < 7; ++i) xw[i] = wp[8 * i];
  SB0;
  SB0;
  const u32x4* up = (const u32x4*)(P.u + (size_t)(64 * s + r) * D_) + c8;
#pragma unroll
  for (int i = 0; i < 7; ++i) xu[i] = up[8 * i];
#pragma unroll
  for (int i = 0; i < 7; ++i) gw[i] = ((const u32x4*)P.gw)[c8 + 8 * i];
  const u64 tag = ld_flag(P.ws + WS_EPOCH) + 1;
  SB0;
  SB0;
#pragma unroll
  for (int i = 0; i < 7; ++i) *(LAS u32x4*)(lds + 65536 + r * RS + 16 * (c8 + 8 * i)) = xw[i];
  asm volatile("s_waitcnt lgkmcnt(0)\n\ts_barrier" ::: "memory");
  STAMP(2);
  auto rws = mkrs(P.ws, WS_END);
  const int q = lane >> 4, a = (lane & 15) >> 2, p = lane & 3;
  u32 wbase = dow ? WS_WD + 8192 * s + lane * 16 : WS_END;
  asm volatile("" : "+v"(wbase));
#pragma unroll
  for (int i = 0; i < 7; ++i) {
    const int un = wave * 7 + i, dt = un >> 1, kh = un & 1;
    const u32 ad = 65536 + (32 * kh + 16 * (q >> 1) + 4 * (q & 1) + a) * RS + 32 * dt + 8 * p;
    __builtin_amdgcn_raw_buffer_store_b128(__builtin_bit_cast(u32x4, tr_frag(lds, ad, ad + 8 * RS)), rws,
        wbase + (((dt >> 2) * 56 + kh) * 4 + (dt & 3)) * 1024, 0, 16);
  }
  SB0;
  STAMP(3);
#pragma unroll
  for (int i = 0; i < 7; ++i) *(LAS u32x4*)(lds + r * RS + 16 * (c8 + 8 * i)) = xu[i];
  asm volatile("s_waitcnt vmcnt(0) lgkmcnt(0)\n\ts_barrier" ::: "memory");
  if (tid == 0) st_flag(P.ws + WS_FW + 128 * s, tag);
  STAMP(4);
#pragma unroll
  for (int i = 0; i < 7; ++i) {
    const int d8 = 8 * (wave * 7 + i);
    const u32 ad = (8 * (2 * q + (p >> 1)) + a) * RS + 2 * d8 + 8 * (p & 1);
    u32x4 v = __builtin_bit_cast(u32x4, tr_frag(lds, ad, ad + 4 * RS));
    LAS char* st = lds + 65536 + 1152 * (wave * 7 + i);
    *(LAS u32x4*)(st + 144 * (lane & 7) + 16 * (2 * q + ((lane >> 3) & 1))) = v;
    v = *(LAS u32x4*)(st + 144 * (lane >> 3) + 16 * (lane & 7));
    __builtin_amdgcn_raw_buffer_store_b128(v, rws, WS_UT + (d8 + (lane >> 3)) * TS + 128 * s + 16 * (lane & 7), 0, 16);
  }
  asm volatile("s_waitcnt vmcnt(0)\n\ts_barrier" ::: "memory");
  if (tid == 0) st_flag(P.ws + WS_FU + 128 * s, tag);
  STAMP(5);
  float gs = 0.f;
#pragma unroll
  for (int i = 0; i < 7; ++i)
#pragma unroll
    for (int j = 0; j < 4; ++j) {
      const u32 pr = pk_bf16(bf_lo(xu[i][j]) * bf_lo(gw[i][j]), bf_hi(xu[i][j]) * bf_hi(gw[i][j]));
      gs += bf_lo(pr) + bf_hi(pr);
    }
  gs += __shfl_xor(gs, 1);
  gs += __shfl_xor(gs, 2);
  gs += __shfl_xor(gs, 4);
  if (c8 == 0) {
    const float g = rbf(__builtin_amdgcn_rcpf(1.f + __expf(-rbf(gs))));
    __builtin_amdgcn_raw_buffer_store_b32(__float_as_uint(g), rws, WS_G + 4 * (64 * s + r), 0, 16);
  }
  asm volatile("s_waitcnt vmcnt(0)" ::: "memory");
  __syncthreads();
  if (tid == 0) st_flag(P.ws + WS_FG + 128 * s, tag);
  STAMP(1);
  STAMP_OUT;
  if (s == 0 && wave == 0) {
#pragma unroll 1
    for (int i = 0; i < 4; ++i) wait_flags(P.ws + WS_FG + 128 * (lane + 64 * i), tag);
    if (lane == 0) st_flag(P.ws + WS_EPOCH, tag);
  }
}

#define MF32(acc, a, b) acc = __builtin_amdgcn_mfma_f32_32x32x16_bf16(a, b, acc, 0, 0, 0)
#define MF16(acc, a, b) acc = __builtin_amdgcn_mfma_f32_16x16x32_bf16(a, b, acc, 0, 0, 0)
#define MFH(acc, a, b) acc = __builtin_amdgcn_mfma_f32_16x16x32_f16(a, b, acc, 0, 0, 0)
#define YOUNG_FIRST(n) do { if ((PRI >> (n)) & 1) asm volatile("s_cmp_lt_u32 %0, 4\n\ts_cbranch_scc1 1f\n\ts_setprio 1\n1:" :: "s"(wave) : "scc"); } while (0)
#define PRIO_OFF(n) do { if ((PRI >> (n)) & 1) __builtin_amdgcn_s_setprio(0); } while (0)
#define PRI 0
#define BARL do { SB0; asm volatile("s_waitcnt lgkmcnt(0)\n\ts_barrier" ::: "memory"); SB0; } while (0)
template <int N> DI void wait_vm() { asm volatile("s_waitcnt vmcnt(%0)" :: "n"(N) : "memory"); }
template <int N> struct IC { static constexpr int v = N; };

struct G1 {
  __amdgpu_buffer_rsrc_t rx, rg, ru;
  u32 vx[2], vw[2], wofs;
  u32 xa[3][4], wa[3][2];
};
struct G1Frag { s16x8 a[2], b[2]; };
template <int C, int I>
DI void g1_dma(const G1& d) {
  if constexpr (C < 7) {
    const u32 dst = (C % 3) * 49152u + (I >> 1) * 16384u + (I & 1) * 1024u + d.wofs;
    if constexpr (I < 2) dma16(dst, d.vx[I], d.rx, C * 128u);
    else dma16(dst, d.vw[I & 1], I < 4 ? d.rg : d.ru, C * 64u * 3584u);
  }
}
template <int C, int KS, int I>
DI void g1_rd(G1Frag& f, LAS char* lds, const G1& d) {
  if constexpr (C < 7) {
    if constexpr (I == 0) {
      f.b[0] = *(LAS s16x8*)(lds + d.xa[C % 3][KS]);
      f.b[1] = *(LAS s16x8*)(lds + d.xa[C % 3][KS] + 4096u);
    } else {
      f.a[I - 1] = tr_frag(lds, d.wa[C % 3][0] + I * 16384u + KS * 4096u, d.wa[C % 3][1] + I * 16384u + KS * 4096u);
    }
  }
}
#define PAD1 0
#define PAD2 0
#define PAD4 0
#define PADN(n) do { if ((n) > 0) { SB0; asm volatile("s_nop %0" :: "n"((n) > 16 ? 15 : (n) - 1)); if ((n) > 16) asm volatile("s_nop %0" :: "n"((n) > 16 ? (n) - 17 : 0)); } } while (0)
template <int S, class X>
DI void g1_step(f32x16 (&acc)[2][2], G1Frag (&f)[3], LAS char* lds, G1& d, X&& extra) {
  constexpr int NC = (S + 2) >> 2, NK = (S + 2) & 3;
  constexpr int X0 = S < 2 ? 12 + 3 * S : ((S - 2) & 3) < 3 ? 6 * (((S - 2) >> 2) + 3) + 2 * ((S - 2) & 3) : 99;
  constexpr int X2 = S < 2 ? X0 + 2 : 99;
  G1Frag& cur = f[S % 3];
  G1Frag& nxt = f[(S + 2) % 3];
  extra(IC<4 * S>{});
  SB0;
  MF32(acc[0][0], cur.a[0], cur.b[0]); SB0;
  g1_rd<NC, NK, 0>(nxt, lds, d);
  g1_dma<X0 / 6, X0 % 6>(d);
  extra(IC<4 * S + 1>{}); PADN(PAD1); SB0;
  MF32(acc[0][1], cur.a[0], cur.b[1]); SB0;
  g1_rd<NC, NK, 1>(nxt, lds, d);
  g1_dma<X2 / 6, X2 % 6>(d);
  extra(IC<4 * S + 2>{}); PADN(PAD2); SB0;
  MF32(acc[1][0], cur.a[1], cur.b[0]); SB0;
  g1_rd<NC, NK, 2>(nxt, lds, d);
  g1_dma<(X0 + 1) / 6, (X0 + 1) % 6>(d);
  extra(IC<4 * S + 3>{}); PADN(PAD1); SB0;
  MF32(acc[1][1], cur.a[1], cur.b[1]); SB0;
  extra(IC<1000 + S>{}); PADN(PAD4); SB0;
}
template <int C, class X>
DI void g1_chunk(f32x16 (&acc)[2][2], G1Frag (&f)[3], LAS char* lds, G1& d, X&& extra) {
  g1_step<4 * C>(acc, f, lds, d, extra);
  g1_step<4 * C + 1>(acc, f, lds, d, extra);
  g1_step<4 * C + 2>(acc, f, lds, d, extra);
  g1_step<4 * C + 3>(acc, f, lds, d, extra);
}

}

#define ROT 0
#define NWARM 1
#define LW_STEP 2
#define LW_EARLY 20
#define HPUB 6
#define LDWD(k) wdf[(k) >> 2][(k) & 3] = __builtin_bit_cast(s16x8, __builtin_amdgcn_raw_buffer_load_b128( \
    rws, lane * 16, WS_WD + ((cb * 56 + 7 * wave) * 4 + (k)) * 1024, 16))
#define FHP 6
#define G2_PRE0 13
#define EPI_OFF 2
#define HAUX 16
#define G2_PRE1 G2_PRE0
#define FAP 21
#define FAC 26
#define TMODE 0
#define FHE 0
#define FHC 5
#define G2_PREC 6
#define G2_LA 7
#define D0POS 2
#define D1E 0
#define KPRE 1
#define D1KS 3
#define CP_EPI 4
#define APUB 3
#define G2_PRE 14

__global__ __launch_bounds__(512, 2) void gated_mix_kernel(Params P) {
  __shared__ __attribute__((aligned(16))) char smem[163840];
  LAS char* lds = (LAS char*)smem;
  const int tid = threadIdx.x, lane = tid & 63;
  const int wave = __builtin_amdgcn_readfirstlane(tid >> 6);
  const int bid = blockIdx.x;
  STAMP_DECL;
  STAMP(0);
  if (bid < 32) {
    helper_block(P, lds, bid, tid, lane, wave);
    return;
  }
  const int xcd = bid & 7, slot = (bid >> 3) - 4;
  const int tb = 4 * (xcd >> 1) + slot / 7, fb = 7 * (xcd & 1) + slot % 7;
  const int mid = tb * 14 + fb;
  const int rb = 2 * tb + (xcd & 1), cb = slot % 7;
  const int ch = wave >> 2, d0 = 2 * mid;
  const int q = lane >> 4, r16 = lane & 15;

  const u32 tbl = LD_TB + TB_STRIDE * wave;
  {
    u64 po = mid * 1024 + wave * 128 + (lane & 7) * 16;
    u32 m0v = tbl + 4096;
    asm volatile("" : "+v"(po), "+s"(m0v));
    SB0;
    u32 lo, hi;
#define LO(p) "s"((u32)(u64)(p))
#define HI(p) "s"((u32)((u64)(p) >> 32))
    asm volatile("v_mov_b32 %0, %2\n\tv_mov_b32 %1, %3\n\ts_lshl_b64 exec, exec, 8\n\t"
                 "v_mov_b32 %0, %4\n\tv_mov_b32 %1, %5\n\ts_lshl_b64 exec, exec, 8\n\t"
                 "v_mov_b32 %0, %6\n\tv_mov_b32 %1, %7\n\ts_lshl_b64 exec, exec, 8\n\t"
                 "v_mov_b32 %0, %8\n\tv_mov_b32 %1, %9\n\ts_lshl_b64 exec, exec, 8\n\t"
                 "v_mov_b32 %0, %10\n\tv_mov_b32 %1, %11\n\ts_lshl_b64 exec, exec, 8\n\t"
                 "v_mov_b32 %0, %12\n\tv_mov_b32 %1, %13\n\ts_mov_b64 exec, -1"
                 : "=&v"(lo), "=&v"(hi)
                 : LO(P.are), HI(P.are), LO(P.aim), HI(P.aim), LO(P.bre), HI(P.bre), LO(P.bim), HI(P.bim), LO(P.cre), HI(P.cre),
                   LO(P.cim), HI(P.cim) : "scc");
    asm volatile("s_mov_b32 m0, %0\n\ts_nop 0\n\tglobal_load_lds_dwordx4 %1, off"
                 :: "s"(m0v), "v"((((u64)hi << 32) | lo) + po) : "memory");
  }
  u32 wm0;
  if constexpr (NWARM > 0) {
    const int xj = 5 * wave + (lane - 16 < 5 ? lane - 16 : 0), xr = 19 * (slot % 7) + (xj >> 1);
    u32 ow = (16 * wave + 4 * (lane >> 2) + slot / 7) * 3584 + fb * 256 + (lane & 1) * 128;
    u32 ox = (128 * tb + (xr < 128 ? xr : 127)) * 896 + 128 * (xj & 1);
    asm volatile("" : "+v"(ow), "+v"(ox));
    const u32 o0 = lane < 16 ? ow : ox;
    u32 lo, hi;
    asm volatile("v_mov_b32 %0, %2\n\tv_mov_b32 %1, %3\n\ts_mov_b64 exec, 0x3333\n\tv_mov_b32 %0, %4\n\tv_mov_b32 %1, %5\n\t"
                 "s_mov_b64 exec, 0xcccc\n\tv_mov_b32 %0, %6\n\tv_mov_b32 %1, %7\n\ts_mov_b64 exec, -1"
                 : "=&v"(lo), "=&v"(hi) : LO(P.u), HI(P.u), LO(P.wg), HI(P.wg), LO(P.wu), HI(P.wu));
    asm volatile("s_mov_b64 exec, 0x1fffff\n\tglobal_load_dword %0, %1, off\n\ts_mov_b64 exec, -1"
                 : "=v"(wm0) : "v"((((u64)hi << 32) | lo) + o0) : "memory");
  }
  u64 tag = *(const CAS u64*)(P.ws + WS_EPOCH);
  const u32 p_ldt = *(const CAS u32*)(P.ldt + d0), p_dsk = *(const CAS u32*)(P.dsk + d0);
  SB0;
  G1 d;
  d.rx = mkrs(P.u + (size_t)tb * 128 * D_, 128 * D_ * 2);
  d.rg = mkrs(P.wg + fb * 128, (D_ * F_ - fb * 128) * 2);
  d.ru = mkrs(P.wu + fb * 128, (D_ * F_ - fb * 128) * 2);
  d.wofs = 2048u * wave;
#pragma unroll
  for (int i = 0; i < 2; ++i) {
    d.vx[i] = (16 * wave + 8 * i + (lane >> 3)) * 896 + 16 * ((lane & 7) ^ ((4 * i + q) & 7));
    d.vw[i] = (8 * wave + 4 * i + q) * 3584 + 16 * (r16 ^ ((q << 2) | ((2 * wave + i) & 3)));
  }
  if constexpr (D0POS == 0) {
  g1_dma<0, 0>(d); g1_dma<0, 2>(d); g1_dma<0, 4>(d); g1_dma<0, 1>(d); g1_dma<0, 3>(d); g1_dma<0, 5>(d);
  if constexpr (D1E > 0) g1_dma<1, 0>(d);
  if constexpr (D1E > 1) g1_dma<1, 1>(d);
  }
  auto rws = mkrs(P.ws, WS_END);
  STAMP(14);
  const int wf = wave & 3, wt = wave >> 2, hh = lane >> 5;
#pragma unroll
  for (int st = 0; st < 1; ++st) {
#pragma unroll
    for (int ks = 0; ks < 4; ++ks) {
      d.xa[st][ks] = 49152 * st + 128 * (64 * wt + (lane & 31)) + 16 * ((2 * ks + hh) ^ ((lane >> 1) & 7));
      asm volatile("" : "+v"(d.xa[st][ks]));
    }
    const int blk = q & 1, a = r16 >> 2, p = lane & 3;
#pragma unroll
    for (int t = 0; t < 2; ++t) {
      d.wa[st][t] = 49152 * st + 256 * (8 * hh + 4 * t + a) + 16 * ((4 * wf + 2 * blk + (p >> 1)) ^ ((a << 2) | ((2 * hh + t) & 3))) + 8 * (p & 1);
      asm volatile("" : "+v"(d.wa[st][t]));
    }
  }
  const int tl = tid & 255;
  u32 kofs[8], ra[7], wr[5];
  {
    const int hi = 4 * (r16 >> 2), lo = 4 * (r16 & 3);
    const u32 row[7] = {14 + (u32)(r16 >> 2), (0xd847u >> lo) & 15, (0xa317u >> hi) & 15, (0xb967u >> lo) & 15, (0x7717u >> hi) & 15,
                        (0xc022u >> hi) & 15, 5};
#pragma unroll
    for (int i = 0; i < 7; ++i) {
      ra[i] = tbl + 288 * row[i] + 16 * q;
      asm volatile("" : "+v"(ra[i]));
    }
#pragma unroll
    for (int i = 0; i < 5; ++i) {
      wr[i] = tbl + 4 * lane + 4 * 288 * i;
      asm volatile("" : "+v"(wr[i]));
    }
  }
  const float dt = rbf(dexpf(ch ? bf_hi(p_ldt) : bf_lo(p_ldt)));
#define KPUT(row, v) *(LAS u32*)(lds + wr[(row) >> 2] + 288 * ((row) & 3)) = (v)
  SB0;
  if constexpr (D0POS == 1) {
  g1_dma<0, 0>(d); g1_dma<0, 2>(d); g1_dma<0, 4>(d); g1_dma<0, 1>(d); g1_dma<0, 3>(d); g1_dma<0, 5>(d);
  if constexpr (D1E > 0) g1_dma<1, 0>(d);
  if constexpr (D1E > 1) g1_dma<1, 1>(d);
  }
  if constexpr (D0POS == 2) {
    if (wave < 4) {
#pragma unroll
      for (int hf2 = 0; hf2 < 2; ++hf2) {
        dma16(d.wofs + 8192 * hf2, d.vx[0], d.rx, hf2 * 64 * 896);
        dma16(16384 + d.wofs + 8192 * hf2, d.vw[0], d.rg, hf2 * 32 * 3584);
        dma16(32768 + d.wofs + 8192 * hf2, d.vw[0], d.ru, hf2 * 32 * 3584);
        dma16(1024 + d.wofs + 8192 * hf2, d.vx[1], d.rx, hf2 * 64 * 896);
        dma16(1024 + 16384 + d.wofs + 8192 * hf2, d.vw[1], d.rg, hf2 * 32 * 3584);
        dma16(1024 + 32768 + d.wofs + 8192 * hf2, d.vw[1], d.ru, hf2 * 32 * 3584);
      }
      wait_vm<NWARM + 12>();
    } else {
      wait_vm<NWARM>();
    }
  }
  STAMP(15);
  SB0;
  if constexpr (D0POS != 2) wait_vm<NWARM + 6 + D1E>();
  u32 prm[6];
#pragma unroll
  for (int i = 0; i < 6; ++i) prm[i] = *(LAS u16*)(lds + tbl + 4096 + 128 * i + 2 * lane);
  SB0;
  STAMP(16);
  f32x4 kq[3] = {};
  u32x4 kx[4][7], kt0, ktc, kfa, kt1;
  float wunv, klv;
  auto kblock = [&](auto I) {
    constexpr int B = decltype(I)::v, ks = B / 5, g = B % 5;
    const u32x4(&c)[7] = kx[ks];
    if constexpr (g == 0) kt0 = cmulh4(c[2], c[3]);
    if constexpr (g == 1) ktc = cmulh4(c[4], c[3]);
    if constexpr (g == 2) kfa = cmulh4(c[0], c[1]);
    if constexpr (g == 3) { kt1 = cmulh4(kt0, c[6]); SB0; MFH(kq[0], H8(kfa), H8(kt0)); }
    if constexpr (g == 4) {
      const u32x4 t2 = cmulh4(ktc, c[5]);
      SB0;
      MFH(kq[1], H8(kfa), H8(kt1));
      asm volatile("s_nop 0");
      SB0;
      MFH(kq[2], H8(kfa), H8(t2));
    }
    if constexpr (B == 0) klv = dpp_add<0x111, 0xf>(klv);
    if constexpr (B == 1) klv = dpp_add<0x112, 0xf>(klv);
    if constexpr (B == 2) klv = dpp_add<0x114, 0xf>(klv);
    if constexpr (B == 5) klv = dpp_add<0x118, 0xf>(klv);
    if constexpr (B == 6) klv = dpp_add<0x142, 0xa>(klv);
    if constexpr (B == 7) klv = dpp_add<0x143, 0xc>(klv);
    SB0;
  };
  {
    const float a2 = (dt * bf_lo(prm[0])) * 1.4426950408889634f, b2 = (dt * bf_lo(prm[1])) * 0.15915494309189535f;
    const float br = dt * bf_lo(prm[2]), bi = dt * bf_lo(prm[3]), cr = bf_lo(prm[4]), ci = bf_lo(prm[5]);
    const f32x2 v = {cr * br - ci * bi, -(cr * bi + ci * br)};
    const float f1 = __builtin_amdgcn_fractf(-b2), f16 = __builtin_amdgcn_fractf(-16.f * b2), f256 = __builtin_amdgcn_fractf(256.f * b2);
    u32 wm = __builtin_bit_cast(u32, __builtin_fmaxf(__builtin_fabsf(v.x), __builtin_fabsf(v.y)));
    const float e1 = __builtin_amdgcn_exp2f(a2);
    SBV;
    wm = dpp_umax<0x111, 0xf>(wm);
    const float e16 = __builtin_amdgcn_exp2f(16.f * a2), e256 = __builtin_amdgcn_exp2f(256.f * a2);
    SBV;
    wm = dpp_umax<0x112, 0xf>(wm);
    const float c1 = __builtin_amdgcn_cosf(f1), s1 = __builtin_amdgcn_sinf(f1);
    SBV;
    wm = dpp_umax<0x114, 0xf>(wm);
    const float c16 = __builtin_amdgcn_cosf(f16), s16 = __builtin_amdgcn_sinf(f16);
    SBV;
    wm = dpp_umax<0x118, 0xf>(wm);
    const float c256 = __builtin_amdgcn_cosf(f256), s256 = __builtin_amdgcn_sinf(f256);
    SBV;
    wm = dpp_umax<0x142, 0xa>(wm);
    const f32x2 z1 = f32x2{c1, s1} * e1, z16 = f32x2{c16, s16} * e16, z256 = f32x2{c256, s256} * e256;
    SBV;
    wm = dpp_umax<0x143, 0xc>(wm);
    f32x2 l1[4], l2[4], l3[4], l4[4];
    cmul4(l1, {z1, z256, v, z16}, {z1, z256, z16, z16});
    SBV;
    KPUT(0, cvth_conj(z1)); KPUT(1, cvth(z256)); KPUT(7, 0x3c00u);
    u32 we = __builtin_amdgcn_readlane(wm, 63) >> 23;
    SBV;
    cmul4(l2, {l1[0], l1[1], l1[2], l1[3]}, {l1[0], l1[1], z16, l1[3]});
    SBV;
    KPUT(2, cvth_conj(l1[0])); KPUT(3, cvth(l1[1]));
    we = we < 1 ? 1 : we > 253 ? 253 : we;
    const float wsc = __builtin_bit_cast(float, (254 - we) << 23), wun = __builtin_bit_cast(float, we << 23);
    SBV;
    cmul4(l3, {l2[0], l2[1], l2[2], l2[3]}, {l2[0], l2[1], z16, l2[3]});
    SBV;
    KPUT(4, cvth(l2[0])); KPUT(5, cvth(l2[1])); KPUT(6, cvth_conj(l2[3]));
    KPUT(14, cvth(v * wsc)); KPUT(15, cvth(l1[2] * wsc));
    SBV;
    cmul4(l4, {l1[0], l1[1], l3[0], l3[3]}, {z1, z256, l2[0], l2[3]});
    SBV;
    KPUT(8, cvth(l3[0])); KPUT(9, cvth_conj(l3[3]));
    KPUT(16, cvth(l2[2] * wsc)); KPUT(17, cvth(l3[2] * wsc));
    float kl = v.x * l3[1].x + v.y * l3[1].y;
    SBV;
    KPUT(10, cvth(l4[1])); KPUT(11, cvth_conj(l4[3])); KPUT(12, cvth_conj(l4[0])); KPUT(13, cvth(l4[2]));
    STAMP(17);
    SB0;
    if constexpr (D1E < 1) g1_dma<1, 0>(d);
    if constexpr (D1E < 2) g1_dma<1, 1>(d);
    g1_dma<1, 2>(d); g1_dma<1, 3>(d); g1_dma<1, 4>(d); g1_dma<1, 5>(d);
    STAMP(6);
    wunv = wun;
    klv = kl;
#pragma unroll
    for (int ks = 0; ks < 4; ++ks)
#pragma unroll
      for (int j = 0; j < 7; ++j) kx[ks][j] = *(LAS u32x4*)(lds + ra[j] + 64 * ks);
    SB0;
    kblock(IC<0>{}); kblock(IC<1>{}); kblock(IC<2>{}); kblock(IC<3>{}); kblock(IC<4>{});
    if constexpr (KPRE > 1) { kblock(IC<5>{}); kblock(IC<6>{}); kblock(IC<7>{}); kblock(IC<8>{}); kblock(IC<9>{}); }
    STAMP(18);
  }
  asm volatile("" : "+s"(tag));
  tag += 1;
  u64 tagv = tag;
  asm volatile("" : "+v"(tagv));
  STAMP(19);
  if constexpr (NWARM > 0) asm volatile("s_waitcnt vmcnt(6)" : "+v"(wm0) :: "memory");
  else wait_vm<6>();
  BARL;
  G1Frag fr[3];
  g1_rd<0, 0, 0>(fr[0], lds, d); g1_rd<0, 0, 1>(fr[0], lds, d); g1_rd<0, 0, 2>(fr[0], lds, d);
  g1_rd<0, 1, 0>(fr[1], lds, d); g1_rd<0, 1, 1>(fr[1], lds, d); g1_rd<0, 1, 2>(fr[1], lds, d);
  STAMP(1);

  s16x8 wdf[7][4];
  u32x4 urow;
  const char* fgp = P.ws + WS_FU + 128 * (lane & 31);
  u64 fg;
  f32x16 acc[2][2] = {};
  LAS char* cp = lds + LD_CP + CP_STRIDE * ch;
  LAS char* upl = lds + LD_UP + UP_STRIDE * ch;
  u32x4 klo, khi;
  const int cj = (wave + ch) & 3, wj = wave & 3;
  u32 ab[2], bb[2], exa, exs, fho, exw, exr, exo, uur, ato;
  u32 aa[7], cb0;
  auto kcopy = [&](auto I) {
    constexpr int sh = decltype(I)::v, o = (7 - sh) >> 1;
    const u32 dd[8] = {klo[0], klo[1], klo[2], klo[3], khi[0], khi[1], khi[2], khi[3]};
    *(LAS u32x4*)(cp + (cb0 ^ (16 * sh))) =
        (sh & 1) ? u32x4{dd[o], dd[o + 1], dd[o + 2], dd[o + 3]} : u32x4{aa[o], aa[o + 1], aa[o + 2], aa[o + 3]};
  };
  {
    u64 fu;
    f32x4 kt[4];
    u32 kp4[4], kro = LD_KP + KR_STRIDE * wave + 64 * r16 + 16 * (q ^ ((r16 >> 1) & 3));
    asm volatile("" : "+v"(kro));
    float kg[8];
    u32 zo;
    const char* fup = P.ws + WS_FW + 128 * (((7 * wave) >> 1) + ((lane & 3) < 3 ? (lane & 3) : ((7 * wave + 6) >> 1) - ((7 * wave) >> 1)));

    u32x4 kt2;
    u32 tv0, tv1;
#define PIN1(a) asm volatile("" : "+v"(a))
#define PIN2(a, b) asm volatile("" : "+v"(a), "+v"(b))
#define PIN3(a, b, c) asm volatile("" : "+v"(a), "+v"(b), "+v"(c))
    auto task = [&](auto I) {
      constexpr int N0 = decltype(I)::v;
      if constexpr (N0 == 1 || N0 == 3 || N0 == 17 || N0 == 19) {
        constexpr int st = N0 < 8 ? 1 : 2, h = (N0 >> 1) & 1;
        d.xa[st][2 * h] = d.xa[0][2 * h] + 49152 * st; d.xa[st][2 * h + 1] = d.xa[0][2 * h + 1] + 49152 * st;
        d.wa[st][h] = d.wa[0][h] + 49152 * st;
        PIN3(d.xa[st][2 * h], d.xa[st][2 * h + 1], d.wa[st][h]);
      }
      constexpr int N = N0 - (N0 > 1) - (N0 > 3) - (N0 > 17) - (N0 > 19);
      constexpr bool own = !(N0 == 1 || N0 == 3 || N0 == 17 || N0 == 19);
      constexpr int NK = 10 * (4 - KPRE);
      if constexpr (own && N < NK) {
        constexpr int ks = KPRE + N / 10, g = N % 10;
        const u32x4(&c)[7] = kx[ks];
        if constexpr (g == 0) kt0 = mulh4(c[2], c[3]);
        if constexpr (g == 1) kt0 = fmah4(kt0, c[2], c[3]);
        if constexpr (g == 2) kfa = mulh4(c[0], c[1]);
        if constexpr (g == 3) kfa = fmah4(kfa, c[0], c[1]);
        if constexpr (g == 4) ktc = mulh4(c[4], c[3]);
        if constexpr (g == 5) { ktc = fmah4(ktc, c[4], c[3]); SB0; MFH(kq[0], H8(kfa), H8(kt0)); }
        if constexpr (g == 6) kt1 = mulh4(kt0, c[6]);
        if constexpr (g == 7) kt1 = fmah4(kt1, kt0, c[6]);
        if constexpr (g == 8) kt2 = mulh4(ktc, c[5]);
        if constexpr (g == 9) { kt2 = fmah4(kt2, ktc, c[5]); SB0; MFH(kq[1], H8(kfa), H8(kt1)); }
        if constexpr (g == 0 && N > 0) { SB0; MFH(kq[2], H8(kfa), H8(kt2)); }
        if constexpr (KPRE == 1 && N == 1) klv = dpp_add<0x118, 0xf>(klv);
        if constexpr (KPRE == 1 && N == 3) klv = dpp_add<0x142, 0xa>(klv);
        if constexpr (KPRE == 1 && N == 7) klv = dpp_add<0x143, 0xc>(klv);
      }
      constexpr int M = own ? N - NK : -1;
      if constexpr (M == 0) MFH(kq[2], H8(kfa), H8(kt2));
      if constexpr (M >= 1 && M <= 4) {
        constexpr int i = (M - 1) >> 1;
        const int rho = 2 * cj + i, sft = rho >= 5;
        if constexpr (M & 1) {
          tv0 = 19 - 4 * rho - q;
          bb[i] = LD_UP + UP_STRIDE * ch + 512 + 64 * rho - 512 * sft + 16 * (r16 + q);
          tv1 = 16 * ((lane ^ tv0) & 7);
          PIN3(tv0, tv1, bb[i]);
        } else {
          ab[i] = LD_CP + CP_STRIDE * ch + 4096 * sft + 128 * (2 * (lane & 8) + tv0) + tv1;
          PIN1(ab[i]);
        }
      }
      if constexpr (M >= 5 && M <= 7) {
        kq[M - 5] = mul4(kq[M - 5], wunv);
        if constexpr (M == 6) { if (lane == 63) kq[1][3] = klv; }
      }
      if constexpr (M == 8) { tv0 = (255 - tl) * 0x80008u; tv1 = tl == 0 ? 0x40004u : 0u; PIN2(tv0, tv1); }
      if constexpr (M >= 9 && M <= 12) {
        constexpr int i = M - 9;
        constexpr u32 c0[4] = {2564u << 16 | 3332, 4u << 16 | 3076, 2560u << 16 | 3328, 3072}, c1[4] = {2564u << 16 | 8, 0x40004, 2560u << 16 | 4, 0};
        constexpr u32 c2[4] = {0x80008, 0x40004, 4, 0}, c3[4] = {0x80008, 8, 0, 0};
        u32 lo = wf == 3 ? c1[i] : wf == 2 ? c2[i] : c3[i], hi = wf == 3 ? c0[i] : lo;
        asm volatile("" : "+s"(lo), "+s"(hi));
        kp4[i] = (lane < 32 ? lo : hi) + tv0;
        if constexpr (i < 2) kp4[i] -= tv1;
        PIN1(kp4[i]);
      }
      if constexpr (M == 13) { kp4[1] += wf < 2 ? (~tl & 1) << 19 : 0u; PIN1(kp4[1]); }
      if constexpr (M >= 14 && M <= 17) {
        constexpr int i = M - 14;
        const u32 t = kp4[i] ^ ((kp4[i] >> 3) & 0x300030u);
        kofs[2 * i] = LD_KR + KR_STRIDE * ch + (t & 0xffffu);
        kofs[2 * i + 1] = LD_KR + KR_STRIDE * ch + (t >> 16);
        PIN2(kofs[2 * i], kofs[2 * i + 1]);
      }
      if constexpr (M == 22) { tv0 = 7 * wave + ((lane & 7) < 6 ? (lane & 7) : 6); PIN1(tv0); }
      if constexpr (M == 23) { fho = WS_FH + 128 * tb * 14 + 64 * (xcd & 1) + 32 * (tv0 & ~3u) + 16 * (tv0 & 3); PIN1(fho); }
      if constexpr (M == 18) {
        asm volatile("s_cmp_lt_u32 %2, 3\n\ts_cselect_b32 %0, %3, %4\n\ts_cselect_b32 %1, %5, %6" : "=&s"(exa), "=&s"(exs)
                     : "s"(wj), "s"(LD_EX + 1024 * (3 * ch + wj)), "s"(LD_EXO + 1024 * ch), "s"(6144), "s"(2048) : "scc");
      }
      if constexpr (M == 19) { exw = exa + ((16 * lane) ^ (8 * (lane & 16))); PIN1(exw); }
      if constexpr (M == 20) { tv0 = 16 * (lane >> 2) + 8 * (lane & 1); tv1 = (lane >> 1) & 1; PIN2(tv0, tv1); }
      if constexpr (M == 21) { tv0 = (tv0 ^ (128 * tv1)) + 256 * tv1; exr = LD_EX + 6144 * wave + tv0; exo = LD_EXO + 2048 * wave + tv0; PIN2(exr, exo); }
      if constexpr (M == 24) { fho = lane < 8 ? fho : WS_FG + 128 * rb; PIN1(fho); }
      if constexpr (M == 25) {
        uur = LD_UP + 512 + 512 * wave + 4 * lane;
        ato = WS_AT + mid * AS + 2048 * wave + 16 * lane;
        PIN2(uur, ato);
      }
      if constexpr (M == 27) { zo = LD_CP + CP_STRIDE * ch + 16 * tl + (tl >= 152 ? 275 * 128 - 16 * 152 : 0); PIN1(zo); }
      if constexpr (M == 28) { zo += tl >= 160 ? (LD_UP - LD_CP) - (CP_STRIDE - UP_STRIDE) * ch - 275 * 128 - 16 * 8 : 0; PIN1(zo); }
      if constexpr (M == 29) { zo += tl >= 192 ? 4608 - 16 * 32 : 0; PIN1(zo); }
      if constexpr (M == 30) { cb0 = 128 * (tl + 19) + 16 * ((tl + 19) & 7); PIN1(cb0); }
    };
    auto extra = [&](auto I) {
      constexpr int X = decltype(I)::v, S = X >= 1000 ? 99 : X >> 2, J = X & 3;
      constexpr int SLW = 17;
      if constexpr (J == 0 && (S & 3) == 2 && S < 26) {
        constexpr int nlw = LW_STEP * (S - SLW) < LW_EARLY ? LW_STEP * (S - SLW) : LW_EARLY;
        constexpr int N = S == 14 ? 6 + 1 : S == 18 ? 63 : S == 22 ? nlw : 6;
        asm volatile("s_waitcnt vmcnt(%0) lgkmcnt(0)\n\ts_barrier" :: "n"(N) : "memory");
      }
      if constexpr (TMODE == 0) {
        if constexpr (X >= 1000) task(IC<4 * (X - 1000) + 3>{});
        else if constexpr (J >= 1) task(IC<4 * S + J - 1>{});
      } else if constexpr (TMODE == 1) {
        if constexpr (X >= 1000) { task(IC<4 * (X - 1000) + 2>{}); SB0; task(IC<4 * (X - 1000) + 3>{}); }
        else if constexpr (J == 2) { task(IC<4 * S>{}); SB0; task(IC<4 * S + 1>{}); }
      } else {
        if constexpr (X >= 1000) { task(IC<4 * (X - 1000) + 1>{}); SB0; task(IC<4 * (X - 1000) + 2>{}); SB0; task(IC<4 * (X - 1000) + 3>{}); }
        else if constexpr (J == 2) task(IC<4 * S>{});
      }
      if constexpr (S == 19 && J >= 1) {
        constexpr u32 so[3] = {0, 1024, 2560};
        *(LAS f32x4*)(lds + kro + so[J - 1]) = kq[J - 1];
      }
      if constexpr ((S == 20 || S == 25) && J == 0) asm volatile("s_waitcnt lgkmcnt(0)\n\ts_barrier" ::: "memory");
      if constexpr (S == 20 && J >= 1 && J <= 2) {
#pragma unroll
        for (int i = 2 * J - 2; i < 2 * J; ++i) kt[i] = *(LAS f32x4*)(lds + LD_KP + KR_STRIDE * (4 * ch + i) + 16 * (tl < 224 ? tl : 0));
      }
      if constexpr (S == 21 && J == 1) { kt[0] = add4(kt[0], kt[1]); kt[2] = add4(kt[2], kt[3]); }
      if constexpr (S == 21 && J == 2) kt[0] = add4(kt[0], kt[2]);
      if constexpr (S == 21 && J == 3) { if (tl < 224) *(LAS f32x4*)(lds + LD_KR + KR_STRIDE * ch + 16 * tl) = kt[0]; }
      if constexpr (S == 22 && J >= 1) {
#pragma unroll
        for (int j = J == 1 ? 0 : 3 * J - 4; j < (J == 1 ? 2 : 3 * J - 1); ++j) kg[j] = *(LAS float*)(lds + kofs[j]);
      }
      if constexpr (S == 24 && J == 1)
        *(LAS u32x4*)(lds + LD_RK + 4096 * ch + 16 * tl) =
            u32x4{pk_bf16(kg[0], kg[1]), pk_bf16(kg[2], kg[3]), pk_bf16(kg[4], kg[5]), pk_bf16(kg[6], kg[7])};
      if constexpr (S == 13 && J == 1) asm volatile("global_load_dwordx2 %0, %1, off sc1" : "=v"(fu) : "v"(fup) : "memory");
      if constexpr (S == SLW && J == 0) {
        asm volatile("s_waitcnt vmcnt(6)" : "+v"(fu) :: "memory");
        if (__builtin_expect(!all_eq(fu, tag), 0)) { FALLBACK(0); wait_flags(fup, tag); }
        asm volatile("" ::: "memory");
      }
      if constexpr (S == 26 && J == 2) fg = ld_flag(fgp);
      if constexpr (S >= SLW && J >= 1 && J <= LW_STEP) {
        constexpr int k = LW_STEP * (S - SLW) + J - 1;
        if constexpr (k < LW_EARLY) LDWD(k);
      }
      if constexpr (S == 25 && J == 1) {
        klo = *(LAS u32x4*)(lds + LD_RK + 4096 * ch + 16 * (255 - tl));
        khi = *(LAS u32x4*)(lds + LD_RK + 4096 * ch + 16 * (tl ? 256 - tl : 0));
      }
      if constexpr (S == 23 && J == 2) {
        if (tl < 196) *(LAS u32x4*)(lds + zo) = u32x4{0, 0, 0, 0};
      }
      if constexpr (S == 26 && J == 0) {
        if (tl == 0) khi = u32x4{0, 0, 0, 0};
        const u32 dd[8] = {klo[0], klo[1], klo[2], klo[3], khi[0], khi[1], khi[2], khi[3]};
#pragma unroll
        for (int k = 0; k < 7; ++k) aa[k] = __builtin_amdgcn_alignbit(dd[k + 1], dd[k], 16);
      }
      if constexpr ((S >= 26 && S < 28 && J >= 1) || X == 1026 || X == 1027) {
        constexpr int sl = X == 1026 ? 3 : X == 1027 ? 7 : 4 * (S - 26) + J - 1;
        if constexpr (CP_EPI == 0) kcopy(IC<sl>{});
        else if constexpr (CP_EPI == 4 && (sl & 1) == 0) kcopy(IC<sl / 2>{});
      }
    };
    g1_chunk<0>(acc, fr, lds, d, extra);
    STAMP(2);
    g1_chunk<1>(acc, fr, lds, d, extra);
    STAMP(27);
    g1_chunk<2>(acc, fr, lds, d, extra);
    STAMP(28);
    g1_chunk<3>(acc, fr, lds, d, extra);
    STAMP(3);
    g1_chunk<4>(acc, fr, lds, d, extra);
    STAMP(29);
    g1_chunk<5>(acc, fr, lds, d, extra);
    STAMP(20);
    YOUNG_FIRST(1);
    g1_chunk<6>(acc, fr, lds, d, extra);
  }
  asm volatile("s_nop 7\n\ts_nop 7");
  STAMP(4);
  {
    const u32 hv = WS_H + ((u32)(tb * 8 + 4 * wt + (q & 1)) * 56 + fb * 4 + wf) * 1024 + hh * 256 + r16 * 16;
#pragma unroll
    for (int gi = 0; gi < 4; ++gi) {
      const int tt = gi >> 1, s = gi & 1;
      if (gi == EPI_OFF) PRIO_OFF(1);
      f32x2 g2[4], u2[4], e[4], gv[4], hv2[4];
#pragma unroll
      for (int j = 0; j < 4; ++j) {
        g2[j] = f32x2{acc[0][tt][8 * s + 2 * j], acc[0][tt][8 * s + 2 * j + 1]};
        u2[j] = f32x2{acc[1][tt][8 * s + 2 * j], acc[1][tt][8 * s + 2 * j + 1]};
      }
      pk4_mul_s(e, g2, f32x2{-1.4426950408889634f, -1.4426950408889634f});
      SBV;
      pk4_mul(gv, g2, u2);
      SB0;
      if (CP_EPI == 8) {
        if (gi == 0) kcopy(IC<4>{});
        if (gi == 1) kcopy(IC<5>{});
        if (gi == 2) kcopy(IC<6>{});
        if (gi == 3) kcopy(IC<7>{});
      }
      SB0;
#pragma unroll
      for (int j = 0; j < 4; ++j) e[j] = f32x2{__builtin_amdgcn_exp2f(e[j].x), __builtin_amdgcn_exp2f(e[j].y)};
      SBV;
      pk4_add1(e);
      SB0;
      if (CP_EPI >= 4) {
        if (gi == 0) kcopy(IC<8 - CP_EPI>{});
        if (gi == 1) kcopy(IC<9 - CP_EPI>{});
        if (gi == 2) kcopy(IC<10 - CP_EPI>{});
        if (gi == 3) kcopy(IC<11 - CP_EPI>{});
      }
      SB0;
#pragma unroll
      for (int j = 0; j < 4; ++j) e[j] = f32x2{__builtin_amdgcn_rcpf(e[j].x), __builtin_amdgcn_rcpf(e[j].y)};
      SBV;
      asm volatile("s_nop 0");
      pk4_mul(hv2, gv, e);
      SBV;
      const u32x4 o = {pk_bf16(hv2[0]), pk_bf16(hv2[1]), pk_bf16(hv2[2]), pk_bf16(hv2[3])};
      SBV;
      if (gi == 0) {
        if (__builtin_expect(!all_eq(fg, tag), 0)) { FALLBACK(1); wait_flags(fgp, tag); }
        asm volatile("" ::: "memory");
        urow = __builtin_amdgcn_raw_buffer_load_b128(rws, 16 * tl, WS_UT + (d0 + ch) * TS, 16);
      }
      __builtin_amdgcn_raw_buffer_store_b128(o, rws, hv + tt * 2 * 56 * 1024 + s * 512, 0, 16);
    }
  }
  *(LAS u32x4*)(upl + 512 + 16 * tl) = urow;
  STAMP(5);
  YOUNG_FIRST(2);
  BARL;
  STAMP(11);
  u64 fh;
  u32 ppa[4], avo, gv;
    s16x8 hf[28];
    const u32 hs = WS_H + ((u32)rb * 4 * 56 + 7 * wave) * 1024;
    u32 hst[4];
#pragma unroll
    for (int i = 0; i < 4; ++i) hst[i] = hs + ((i + ROT * cb) & 3) * 56 * 1024;
#define LDH(i) hf[i] = __builtin_bit_cast(s16x8, __builtin_amdgcn_raw_buffer_load_b128(rws, lane * 16 + ((i) % 7 & 3) * 1024, hst[(i) / 7] + ((i) % 7 >> 2) * 4096, HAUX))
  {
    f32x4 ya[8] = {};
#define RDA(i, e) *(LAS s16x8*)(lds + ab[i] + 4096 * (e))
#define RDB(i, p) *(LAS s16x8*)(lds + bb[i] + 512 * (p))
#define LWC(e) (LW_EARLY + ((28 - LW_EARLY) * (HPUB - (e)) + HPUB - 1) / HPUB)
    s16x8 a0[8], b0[8], a1[8], b1[8];
    bool hok = false;
#pragma unroll
    for (int e = 7; e >= 0; --e) { a0[e] = RDA(0, e); b0[7 - e] = RDB(0, 7 - e); }
    SB0;
#pragma unroll
    for (int e = 7; e >= 0; --e) {
#pragma unroll
      for (int jt = e; jt < 8; ++jt) MF16(ya[jt], a0[e], b0[jt - e]);
      SB0;
      if (e < 3) { a1[2 - e] = RDA(1, 2 - e); b1[2 - e] = RDB(1, 2 - e); }
      if (e < HPUB) {
#pragma unroll
        for (int k = LWC(e + 1); k < LWC(e); ++k) LDWD(k);
      }
      if (e == FHE) fh = __builtin_bit_cast(u64, __builtin_amdgcn_raw_buffer_load_b64(rws, fho, 0, 16));
      if (e == 2) {
#pragma unroll
    for (int i = 0; i < 4; ++i) {
      ppa[i] = 4096 * wave + 256 * r16 + 16 * ((4 * i + q) ^ r16) + 49152;
      asm volatile("" : "+v"(ppa[i]));
    }
      }
      if (e == 1) {
        avo = WS_AT + (u32)(32 * cb + (tid >> 4)) * AS + 512 * rb + 16 * (tid & 15);
        asm volatile("" : "+v"(avo));
      }
      if (e == HPUB) {
        STAMP(21);
        publish<0>(rws, WS_FH + 128 * mid + 16 * wave, tagv);
        STAMP(22);
      }
      SB0;
    }
    PRIO_OFF(2);
    if (cj == 2) {
      const s16x8 bm = *(LAS s16x8*)(lds + bb[0] - 512);
#pragma unroll
      for (int jt = 0; jt < 8; ++jt) MF16(ya[jt], RDA(0, jt + 1), bm);
    }
    SB0;
#pragma unroll
    for (int jt = 0; jt < 8; ++jt) {
#pragma unroll
      for (int e = 0; e <= jt; ++e) MF16(ya[jt], a1[e], b1[jt - e]);
      SB0;
      if (jt < 5) { a1[jt + 3] = RDA(1, jt + 3); b1[jt + 3] = RDB(1, jt + 3); }
      if (jt == FHC) {
        STAMP(9);
        asm volatile("" : "+v"(fh) :: "memory");
        hok = all_eq(fh, tag);
      }
      if (jt >= FHC) {
#pragma unroll
        for (int i = G2_PREC * (jt - FHC) / (8 - FHC); i < G2_PREC * (jt + 1 - FHC) / (8 - FHC); ++i) { LDH(i); SB0; }
      }
      if (jt > 0) {
        *(LAS f32x4*)(lds + exw) = ya[jt - 1];
        exw += exs;
      }
      SB0;
    }
    *(LAS f32x4*)(lds + exw) = ya[7];
    STAMP(7);
    if (__builtin_expect(!hok, 0)) {
      FALLBACK(2);
      wait_flags(P.ws + fho, tag);
#pragma unroll
      for (int i = 0; i < G2_PREC; ++i) { LDH(i); SB0; }
    }
#pragma unroll
    for (int i = G2_PREC; i < G2_PRE0; ++i) { LDH(i); SB0; }
    gv = __builtin_amdgcn_raw_buffer_load_b32(rws, 4 * lane, WS_G + 256 * rb, 16);
    STAMP(26);
    BARL;
    f32x2 ev[2][2][4];
    u32 uu[2][2];
    asm volatile("ds_read_b64 %0, %20\n\tds_read_b64 %1, %20 offset:1024\n\tds_read_b64 %2, %20 offset:2048\n\tds_read_b64 %3, %21\n\t"
                 "ds_read_b64 %4, %20 offset:3072\n\tds_read_b64 %5, %20 offset:4096\n\tds_read_b64 %6, %20 offset:5120\n\t"
                 "ds_read_b64 %7, %21 offset:1024\n\tds_read_b32 %16, %22\n\tds_read_b32 %17, %22 offset:4672\n\t"
                 "ds_read_b64 %8, %20 offset:512\n\tds_read_b64 %9, %20 offset:1536\n\tds_read_b64 %10, %20 offset:2560\n\t"
                 "ds_read_b64 %11, %21 offset:512\n\tds_read_b64 %12, %20 offset:3584\n\tds_read_b64 %13, %20 offset:4608\n\t"
                 "ds_read_b64 %14, %20 offset:5632\n\tds_read_b64 %15, %21 offset:1536\n\tds_read_b32 %18, %22 offset:256\n\t"
                 "ds_read_b32 %19, %22 offset:4928\n\ts_waitcnt lgkmcnt(0)"
                 : "=&v"(ev[0][0][0]), "=&v"(ev[0][0][1]), "=&v"(ev[0][0][2]), "=&v"(ev[0][0][3]), "=&v"(ev[0][1][0]),
                   "=&v"(ev[0][1][1]), "=&v"(ev[0][1][2]), "=&v"(ev[0][1][3]), "=&v"(ev[1][0][0]), "=&v"(ev[1][0][1]),
                   "=&v"(ev[1][0][2]), "=&v"(ev[1][0][3]), "=&v"(ev[1][1][0]), "=&v"(ev[1][1][1]), "=&v"(ev[1][1][2]),
                   "=&v"(ev[1][1][3]), "=&v"(uu[0][0]), "=&v"(uu[0][1]), "=&v"(uu[1][0]), "=&v"(uu[1][1])
                 : "v"(exr), "v"(exo), "v"(uur) : "memory");
    static_assert(UP_STRIDE == 4672, "");
#pragma unroll
    for (int h = 0; h < 2; ++h) {
      f32x2 y[2];
#pragma unroll
      for (int c = 0; c < 2; ++c)
        y[c] = ((ev[h][c][0] + ev[h][c][1]) + (ev[h][c][2] + ev[h][c][3])) +
               f32x2{bf_lo(uu[h][c]), bf_hi(uu[h][c])} * (c ? bf_hi(p_dsk) : bf_lo(p_dsk));
      __builtin_amdgcn_raw_buffer_store_b128(u32x4{pk_bf16(y[0].x, y[1].x), (u32)tag, pk_bf16(y[0].y, y[1].y), (u32)tag}, rws,
                                             ato + 1024 * h, 0, 16);
    }
    SB0;
    STAMP(30);
  }
  STAMP(8);
  {
    u32x4 avl[2];
#define LDA() do { avl[0] = __builtin_amdgcn_raw_buffer_load_b128(rws, avo, 0, 16); \
                   avl[1] = __builtin_amdgcn_raw_buffer_load_b128(rws, avo + 256, 0, 16); } while (0)
#define AOK() all_true(avl[0][1] == (u32)tag && avl[0][3] == (u32)tag && avl[1][1] == (u32)tag && avl[1][3] == (u32)tag)
    constexpr u32 pbase[4] = {49152, 81920, 0, 114688};
    bool aok = false;
    SB0;
    f32x4 oa[2][4];
#define PUTP(tt, i) *(LAS f32x4*)(lds + ppa[i] + ((tt) == 1 ? 32768 : 0)) = oa[(tt) & 1][i]
#pragma unroll
    for (int tt = 0; tt < 4; ++tt) {
#pragma unroll
      for (int k = 0; k < 7; ++k) {
        const int i = 7 * tt + k;
#pragma unroll
        for (int dt4 = 0; dt4 < 4; ++dt4) {
          if (k == 0) oa[tt & 1][dt4] = f32x4{0.f, 0.f, 0.f, 0.f};
          MF16(oa[tt & 1][dt4], wdf[k][dt4], hf[i]);
        }
        SB0;
        if (i + G2_LA >= G2_PRE0 && i + G2_LA < 28) LDH(i + G2_LA);
        if (tt > 0 && k < 4) {
          if (tt == 3) ppa[k] -= 49152;
          PUTP(tt - 1, k);
        }
        if (tt == 2 && k == 5) BARL;
        if (i == FAP) LDA();
        if (i == FAC) {
          aok = AOK();
          if (__builtin_expect(!aok, 0)) LDA();
        }
        if (tt == 3 && k >= 3) { ppa[k - 3] += 114688; asm volatile("" : "+v"(ppa[k - 3])); }
        SB0;
      }
      if (tt == 0) STAMP(23);
      if (tt == 1) STAMP(24);
      if (tt == 2) STAMP(25);
    }
    PUTP(3, 0); PUTP(3, 1); PUTP(3, 2); PUTP(3, 3);
    STAMP(10);
    if (__builtin_expect(!aok, 0)) {
      FALLBACK(3);
#pragma unroll 1
      for (int it = 0; it < SPIN_MAX && !AOK(); ++it) LDA();
    }
    *(LAS u32x2*)(lds + LD_AT + 272 * (tid >> 4) + 8 * (tid & 15)) = u32x2{avl[0][0], avl[0][2]};
    *(LAS u32x2*)(lds + LD_AT + 272 * (tid >> 4) + 8 * (tid & 15) + 128) = u32x2{avl[1][0], avl[1][2]};
    if (wave == 0) *(LAS u32*)(lds + LD_AT + 9216 + 4 * lane) = gv;
    const int dh = wave & 1;
    const int tw = ((wave >> 1) + ROT * cb) & 3, t = 64 * rb + 16 * tw + r16;
    f32x4 b0, b1;
    const u32 ro = (wave < 2 ? pbase[0] : wave < 4 ? pbase[1] : wave < 6 ? pbase[2] : pbase[3]) + 256 * r16;
    u32 r0 = ro + 16 * ((8 * dh + 2 * q) ^ r16), r1 = ro + 16 * ((8 * dh + 2 * q + 1) ^ r16);
    asm volatile("" : "+v"(r0), "+v"(r1));
    auto reduce = [&]() {
      f32x4 p[16];
#pragma unroll
      for (int w = 0; w < 8; ++w) { p[2 * w] = *(LAS f32x4*)(lds + 4096 * w + r0); p[2 * w + 1] = *(LAS f32x4*)(lds + 4096 * w + r1); }
      b0 = ((p[0] + p[2]) + (p[4] + p[6])) + ((p[8] + p[10]) + (p[12] + p[14]));
      b1 = ((p[1] + p[3]) + (p[5] + p[7])) + ((p[9] + p[11]) + (p[13] + p[15]));
    };
    if (wave < 4) {
      reduce();
      BARL;
    } else {
      BARL;
      reduce();
    }
    STAMP(12);
    u32 av[4];
#pragma unroll
    for (int i = 0; i < 4; ++i) av[i] = *(LAS u32*)(lds + LD_AT + 272 * (16 * dh + 4 * q + i) + 4 * (16 * tw + r16));
    const float g = *(LAS float*)(lds + LD_AT + 9216 + 4 * (16 * tw + r16));
    const float og = 1.f - g;
    const f32x4 m0 = f32x4{bf_lo(av[0]), bf_hi(av[0]), bf_lo(av[1]), bf_hi(av[1])} * g + b0 * og;
    const f32x4 m1 = f32x4{bf_lo(av[2]), bf_hi(av[2]), bf_lo(av[3]), bf_hi(av[3])} * g + b1 * og;
    const u32x4 o = {pk_bf16(m0.xy), pk_bf16(m0.zw), pk_bf16(m1.xy), pk_bf16(m1.zw)};
    *(u32x4*)(P.out + (size_t)t * D_ + 64 * cb + 32 * dh + 8 * q) = o;
    STAMP(13);
    STAMP_OUT;
  }
}

extern "C" void kernel_launch(void* const* d_in, const int* in_sizes, int n_in, void* d_out, int out_size, void* d_ws,
                              size_t ws_size, hipStream_t stream) {
  Params P;
  P.u = (const u16*)d_in[0];
  P.are = (const u16*)d_in[1];
  P.aim = (const u16*)d_in[2];
  P.bre = (const u16*)d_in[3];
  P.bim = (const u16*)d_in[4];
  P.cre = (const u16*)d_in[5];
  P.cim = (const u16*)d_in[6];
  P.ldt = (const u16*)d_in[7];
  P.dsk = (const u16*)d_in[8];
  P.wg = (const u16*)d_in[10];
  P.wu = (const u16*)d_in[11];
  P.wd = (const u16*)d_in[12];
  P.gw = (const u16*)d_in[13];
  P.out = (u16*)d_out;
  P.ws = (char*)d_ws;
  gated_mix_kernel<<<256, 512, 0, stream>>>(P);
}
```

```cpp
#define PROBE 16
#include <hip/hip_runtime.h>

typedef unsigned short u16;
typedef unsigned int u32;
typedef unsigned long long u64;
typedef __attribute__((ext_vector_type(8))) short s16x8;
typedef __attribute__((ext_vector_type(4))) short s16x4;
typedef __attribute__((ext_vector_type(2))) float f32x2;
typedef __attribute__((ext_vector_type(4))) float f32x4;
typedef __attribute__((ext_vector_type(16))) float f32x16;
typedef __attribute__((ext_vector_type(2))) u32 u32x2;
typedef __attribute__((ext_vector_type(4))) u32 u32x4;
typedef __attribute__((ext_vector_type(2))) __bf16 bf16x2;
#define LAS __attribute__((address_space(3)))
#define GAS __attribute__((address_space(1)))
#define DI __device__ __forceinline__
#define SB0 __builtin_amdgcn_sched_barrier(0)
#define SBV __builtin_amdgcn_sched_barrier(0x8 | 0x4 | 0x10 | 0x80)

namespace {
constexpr int L_ = 2048, D_ = 448, N_ = 256, F_ = 1792;
constexpr u32 WS_EPOCH = 0;
constexpr u32 WS_FW = 1024;
constexpr u32 WS_FU = WS_FW + 32 * 128;
constexpr u32 WS_FG = WS_FU + 32 * 128;
constexpr u32 WS_FH = WS_FG + 32 * 128;
constexpr u32 WS_G = 98304;
constexpr u32 WS_UT = 131072;
constexpr u32 TS = L_ * 2 + 128;
constexpr u32 AS = L_ * 8 + 128;
constexpr u32 WS_AT = WS_UT + D_ * TS;
constexpr u32 WS_WD = WS_AT + 224 * AS;
constexpr u32 WS_H = WS_WD + F_ * D_ * 2;
constexpr u32 WS_END = WS_H + L_ * F_ * 2;
constexpr u32 WS_PROF = 16u << 20;
constexpr u32 LD_TB = 98304, TB_STRIDE = 5184;
constexpr u32 LD_KR = 155648, KR_STRIDE = 3584;
constexpr u32 LD_KP = 49152;
constexpr u32 LD_RK = 147456;
constexpr u32 LD_CP = 49152, CP_STRIDE = 35328;
constexpr u32 LD_UP = 119808, UP_STRIDE = 4672;
constexpr u32 LD_EX = 0, LD_EXO = 131072;
constexpr u32 LD_AT = 147456;
constexpr int SPIN_MAX = 1 << 16;
#define CODE_PREFETCH_LINES 0
struct Params {
  const u16 *u, *are, *aim, *bre, *bim, *cre, *cim, *ldt, *dsk, *wg, *wu, *wd, *gw;
  u16* out;
  char* ws;
};

DI float bf_lo(u32 x) { return __uint_as_float(x << 16); }
DI float bf_hi(u32 x) { return __uint_as_float(x & 0xffff0000u); }
DI u32 pk_bf16(float a, float b) {
  f32x2 v = {a, b};
  return __builtin_bit_cast(u32, __builtin_convertvector(v, bf16x2));
}
DI u32 pk_bf16(f32x2 v) { return __builtin_bit_cast(u32, __builtin_convertvector(v, bf16x2)); }
struct cf { float x, y; };
DI cf cmul(cf a, cf b) { return {__builtin_fmaf(a.x, b.x, -(a.y * b.y)), __builtin_fmaf(a.x, b.y, a.y * b.x)}; }
DI cf cexp2pi(float x, float y) {
  const float e = __builtin_amdgcn_exp2f(x), f = __builtin_amdgcn_fractf(y);
  return {e * __builtin_amdgcn_cosf(f), e * __builtin_amdgcn_sinf(f)};
}
typedef __attribute__((ext_vector_type(2))) _Float16 h2;
typedef __attribute__((ext_vector_type(8))) _Float16 h8;
DI h2 pkh(cf a) { f32x2 v = {a.x, a.y}; return __builtin_convertvector(v, h2); }
#define PKM " op_sel_hi:[0,1]\n\t"
#define PKF " op_sel:[1,1,0] op_sel_hi:[1,0,1] neg_lo:[0,1,0]\n\t"
DI u32x4 cmulh4(u32x4 a, u32x4 b) {
  u32 r0, r1, r2, r3;
  const u32 a0 = a[0], a1 = a[1], a2 = a[2], a3 = a[3], b0 = b[0], b1 = b[1], b2 = b[2], b3 = b[3];
  asm("v_pk_mul_f16 %0, %4, %8" PKM "v_pk_mul_f16 %1, %5, %9" PKM "v_pk_mul_f16 %2, %6, %10" PKM "v_pk_mul_f16 %3, %7, %11" PKM
      "v_pk_fma_f16 %0, %4, %8, %0" PKF "v_pk_fma_f16 %1, %5, %9, %1" PKF "v_pk_fma_f16 %2, %6, %10, %2" PKF
      "v_pk_fma_f16 %3, %7, %11, %3 op_sel:[1,1,0] op_sel_hi:[1,0,1] neg_lo:[0,1,0]"
      : "=&v"(r0), "=&v"(r1), "=&v"(r2), "=&v"(r3)
      : "v"(a0), "v"(a1), "v"(a2), "v"(a3), "v"(b0), "v"(b1), "v"(b2), "v"(b3));
  return u32x4{r0, r1, r2, r3};
}
DI u32x4 mulh4(u32x4 a, u32x4 b) {
  u32 r0, r1, r2, r3;
  const u32 a0 = a[0], a1 = a[1], a2 = a[2], a3 = a[3], b0 = b[0], b1 = b[1], b2 = b[2], b3 = b[3];
  asm("v_pk_mul_f16 %0, %4, %8" PKM "v_pk_mul_f16 %1, %5, %9" PKM "v_pk_mul_f16 %2, %6, %10" PKM "v_pk_mul_f16 %3, %7, %11 op_sel_hi:[0,1]"
      : "=&v"(r0), "=&v"(r1), "=&v"(r2), "=&v"(r3)
      : "v"(a0), "v"(a1), "v"(a2), "v"(a3), "v"(b0), "v"(b1), "v"(b2), "v"(b3));
  return u32x4{r0, r1, r2, r3};
}
DI u32x4 fmah4(u32x4 r, u32x4 a, u32x4 b) {
  u32 r0 = r[0], r1 = r[1], r2 = r[2], r3 = r[3];
  const u32 a0 = a[0], a1 = a[1], a2 = a[2], a3 = a[3], b0 = b[0], b1 = b[1], b2 = b[2], b3 = b[3];
  asm("v_pk_fma_f16 %0, %4, %8, %0" PKF "v_pk_fma_f16 %1, %5, %9, %1" PKF "v_pk_fma_f16 %2, %6, %10, %2" PKF
      "v_pk_fma_f16 %3, %7, %11, %3 op_sel:[1,1,0] op_sel_hi:[1,0,1] neg_lo:[0,1,0]"
      : "+v"(r0), "+v"(r1), "+v"(r2), "+v"(r3)
      : "v"(a0), "v"(a1), "v"(a2), "v"(a3), "v"(b0), "v"(b1), "v"(b2), "v"(b3));
  return u32x4{r0, r1, r2, r3};
}
DI void cmul4(f32x2 (&r)[4], const f32x2 (&a)[4], const f32x2 (&b)[4]) {
  asm("v_pk_mul_f32 %0, %4, %8" PKM "v_pk_mul_f32 %1, %5, %9" PKM "v_pk_mul_f32 %2, %6, %10" PKM "v_pk_mul_f32 %3, %7, %11" PKM
      "v_pk_fma_f32 %0, %4, %8, %0" PKF "v_pk_fma_f32 %1, %5, %9, %1" PKF "v_pk_fma_f32 %2, %6, %10, %2" PKF
      "v_pk_fma_f32 %3, %7, %11, %3 op_sel:[1,1,0] op_sel_hi:[1,0,1] neg_lo:[0,1,0]"
      : "=&v"(r[0]), "=&v"(r[1]), "=&v"(r[2]), "=&v"(r[3])
      : "v"(a[0]), "v"(a[1]), "v"(a[2]), "v"(a[3]), "v"(b[0]), "v"(b[1]), "v"(b[2]), "v"(b[3]));
}
DI u32 cvth(f32x2 v) { return __builtin_bit_cast(u32, __builtin_convertvector(v, h2)); }
DI u32 cvth_conj(f32x2 v) {
  u32 r;
  const float x = v.x, y = v.y;
  asm("v_cvt_pk_f16_f32 %0, %1, -%2" : "=v"(r) : "v"(x), "v"(y));
  return r;
}
template <int CTRL, int RM> DI u32 dpp_umax(u32 x) {
  const u32 y = __builtin_amdgcn_update_dpp(0u, x, CTRL, RM, 0xf, true);
  return x > y ? x : y;
}
#define H8(v) __builtin_bit_cast(h8, v)
template <int CTRL, int RM> DI float dpp_max(float x) {
  return __builtin_fmaxf(x, __int_as_float(__builtin_amdgcn_update_dpp(0, __float_as_int(x), CTRL, RM, 0xf, true)));
}
template <int CTRL, int RM> DI float dpp_add(float x) {
  return x + __int_as_float(__builtin_amdgcn_update_dpp(0, __float_as_int(x), CTRL, RM, 0xf, true));
}
DI float wave_sum63(float x) {
  x = dpp_add<0x111, 0xf>(x); x = dpp_add<0x112, 0xf>(x); x = dpp_add<0x114, 0xf>(x); x = dpp_add<0x118, 0xf>(x);
  x = dpp_add<0x142, 0xa>(x); x = dpp_add<0x143, 0xc>(x);
  return x;
}
DI float wave_max(float x) {
  x = dpp_max<0x111, 0xf>(x); x = dpp_max<0x112, 0xf>(x); x = dpp_max<0x114, 0xf>(x); x = dpp_max<0x118, 0xf>(x);
  x = dpp_max<0x142, 0xa>(x); x = dpp_max<0x143, 0xc>(x);
  return __int_as_float(__builtin_amdgcn_readlane(__float_as_int(x), 63));
}
DI void pk4_mul_s(f32x2 (&r)[4], const f32x2 (&a)[4], f32x2 c) {
  asm("v_pk_mul_f32 %0, %4, %8\n\tv_pk_mul_f32 %1, %5, %8\n\tv_pk_mul_f32 %2, %6, %8\n\tv_pk_mul_f32 %3, %7, %8"
      : "=&v"(r[0]), "=&v"(r[1]), "=&v"(r[2]), "=&v"(r[3]) : "v"(a[0]), "v"(a[1]), "v"(a[2]), "v"(a[3]), "s"(c));
}
DI void pk4_mul(f32x2 (&r)[4], const f32x2 (&a)[4], const f32x2 (&b)[4]) {
  asm("v_pk_mul_f32 %0, %4, %8\n\tv_pk_mul_f32 %1, %5, %9\n\tv_pk_mul_f32 %2, %6, %10\n\tv_pk_mul_f32 %3, %7, %11"
      : "=&v"(r[0]), "=&v"(r[1]), "=&v"(r[2]), "=&v"(r[3])
      : "v"(a[0]), "v"(a[1]), "v"(a[2]), "v"(a[3]), "v"(b[0]), "v"(b[1]), "v"(b[2]), "v"(b[3]));
}
DI void pk4_add1(f32x2 (&r)[4]) {
  asm("s_nop 0\n\tv_pk_add_f32 %0, %0, 1.0 op_sel_hi:[1,0]\n\tv_pk_add_f32 %1, %1, 1.0 op_sel_hi:[1,0]\n\t"
      "v_pk_add_f32 %2, %2, 1.0 op_sel_hi:[1,0]\n\tv_pk_add_f32 %3, %3, 1.0 op_sel_hi:[1,0]"
      : "+v"(r[0]), "+v"(r[1]), "+v"(r[2]), "+v"(r[3]));
}
DI f32x4 add4(f32x4 a, f32x4 b) {
  float r0, r1, r2, r3;
  const float a0 = a[0], a1 = a[1], a2 = a[2], a3 = a[3], b0 = b[0], b1 = b[1], b2 = b[2], b3 = b[3];
  asm("v_add_f32 %0, %4, %8\n\tv_add_f32 %1, %5, %9\n\tv_add_f32 %2, %6, %10\n\tv_add_f32 %3, %7, %11"
      : "=&v"(r0), "=&v"(r1), "=&v"(r2), "=&v"(r3) : "v"(a0), "v"(a1), "v"(a2), "v"(a3), "v"(b0), "v"(b1), "v"(b2), "v"(b3));
  return f32x4{r0, r1, r2, r3};
}
DI f32x4 mul4(f32x4 a, float b) {
  float r0, r1, r2, r3;
  const float a0 = a[0], a1 = a[1], a2 = a[2], a3 = a[3];
  asm("v_mul_f32 %0, %8, %4\n\tv_mul_f32 %1, %8, %5\n\tv_mul_f32 %2, %8, %6\n\tv_mul_f32 %3, %8, %7"
      : "=&v"(r0), "=&v"(r1), "=&v"(r2), "=&v"(r3) : "v"(a0), "v"(a1), "v"(a2), "v"(a3), "s"(b));
  return f32x4{r0, r1, r2, r3};
}
DI bool all_true(bool p) { return __all(p); }
DI bool all_eq(u64 a, u64 b) { return __all(a == b); }
DI float dexpf(float x) { return expf(x); }
DI float rbf(float x) { return bf_lo(pk_bf16(x, 0.f)); }
DI __amdgpu_buffer_rsrc_t mkrs(const void* p, u32 bytes) {
  return __builtin_amdgcn_make_buffer_rsrc((void*)p, 0, bytes, 0x00020000);
}
DI void dma16(u32 ldsaddr, u32 voff, __amdgpu_buffer_rsrc_t rs, u32 soff) {
  asm volatile("s_mov_b32 m0, %0\n\ts_nop 0\n\tbuffer_load_dwordx4 %1, %2, %3 offen lds"
               :: "s"(ldsaddr), "v"(voff), "s"(rs), "s"(soff) : "memory");
}
DI void gdma4(u32 ldsaddr, u32 voff, const void* base) {
  asm volatile("s_mov_b32 m0, %0\n\ts_nop 0\n\tglobal_load_lds_dword %1, %2" :: "s"(ldsaddr), "v"(voff), "s"(base) : "memory");
}
DI void gdma16(u32 ldsaddr, u32 voff, const void* base) {
  asm volatile("s_mov_b32 m0, %0\n\ts_nop 0\n\tglobal_load_lds_dwordx4 %1, %2" :: "s"(ldsaddr), "v"(voff), "s"(base) : "memory");
}
#define CAS __attribute__((address_space(4)))
DI u64 ld_flag(const char* p) { return __hip_atomic_load((const GAS u64*)p, __ATOMIC_RELAXED, __HIP_MEMORY_SCOPE_AGENT); }
DI void st_flag(char* p, u64 v) { __hip_atomic_store((GAS u64*)p, v, __ATOMIC_RELAXED, __HIP_MEMORY_SCOPE_AGENT); }
DI void wait_flags(const char* p, u64 tag) {
#pragma unroll 1
  for (int it = 0; it < SPIN_MAX; ++it) {
    if (__all(ld_flag(p) == tag)) break;
    __builtin_amdgcn_s_sleep(2);
  }
  asm volatile("" ::: "memory");
}
template <int N> DI void publish(__amdgpu_buffer_rsrc_t rs, u32 off, u64 tagv) {
  asm volatile("s_waitcnt vmcnt(%3)\n\ts_mov_b64 exec, 1\n\tbuffer_store_dwordx2 %0, off, %1, %2 sc1\n\ts_mov_b64 exec, -1"
               :: "v"(tagv), "s"(rs), "s"(off), "n"(N) : "memory");
}
DI s16x8 tr_frag(LAS char* lds, u32 a0, u32 a1) {
  s16x4 x = __builtin_amdgcn_ds_read_tr16_b64_v4i16((LAS s16x4*)(lds + a0));
  s16x4 y = __builtin_amdgcn_ds_read_tr16_b64_v4i16((LAS s16x4*)(lds + a1));
  return __builtin_shufflevector(x, y, 0, 1, 2, 3, 4, 5, 6, 7);
}

#define STAMP(i) do { if ((i) == 0 || (i) == PROBE) { SB0; u64 t_; asm volatile("s_memrealtime %0\n\ts_waitcnt lgkmcnt(0)" : "=s"(t_) :: "memory"); if ((i) == 0) pr0 = t_; else pr1 = t_; SB0; } } while (0)
#define STAMP_DECL u64 pr0 = 0, pr1 = 0
#define FALLBACK(b)
#define PROF_ARGS
#define PROF_PASS
#define STAMP_OUT do { if (blockIdx.x >= 32) { for (int it_ = 0; it_ < 1000000 && (long long)(__builtin_amdgcn_s_memrealtime() - pr1) < 2500; ++it_) __builtin_amdgcn_s_sleep(1); } (void)pr0; } while (0)

DI void helper_block(const Params& P, LAS char* lds, int s, int tid, int lane, int wave) {
  STAMP_DECL;
  STAMP(0);
  constexpr u32 RS = 928;
  const int r = tid >> 3, c8 = tid & 7;
  const bool dow = s < 28;
  u32x4 xw[7], xu[7], gw[7];
  const u32x4* wp = (const u32x4*)(P.wd + (size_t)(64 * (dow ? s : 0) + r) * D_) + c8;
#pragma unroll
  for (int i = 0; i < 7; ++i) xw[i] = wp[8 * i];
  SB0;
  SB0;
  const u32x4* up = (const u32x4*)(P.u + (size_t)(64 * s + r) * D_) + c8;
#pragma unroll
  for (int i = 0; i < 7; ++i) xu[i] = up[8 * i];
#pragma unroll
  for (int i = 0; i < 7; ++i) gw[i] = ((const u32x4*)P.gw)[c8 + 8 * i];
  const u64 tag = ld_flag(P.ws + WS_EPOCH) + 1;
  SB0;
  SB0;
#pragma unroll
  for (int i = 0; i < 7; ++i) *(LAS u32x4*)(lds + 65536 + r * RS + 16 * (c8 + 8 * i)) = xw[i];
  asm volatile("s_waitcnt lgkmcnt(0)\n\ts_barrier" ::: "memory");
  STAMP(2);
  auto rws = mkrs(P.ws, WS_END);
  const int q = lane >> 4, a = (lane & 15) >> 2, p = lane & 3;
  u32 wbase = dow ? WS_WD + 8192 * s + lane * 16 : WS_END;
  asm volatile("" : "+v"(wbase));
#pragma unroll
  for (int i = 0; i < 7; ++i) {
    const int un = wave * 7 + i, dt = un >> 1, kh = un & 1;
    const u32 ad = 65536 + (32 * kh + 16 * (q >> 1) + 4 * (q & 1) + a) * RS + 32 * dt + 8 * p;
    __builtin_amdgcn_raw_buffer_store_b128(__builtin_bit_cast(u32x4, tr_frag(lds, ad, ad + 8 * RS)), rws,
        wbase + (((dt >> 2) * 56 + kh) * 4 + (dt & 3)) * 1024, 0, 16);
  }
  SB0;
  STAMP(3);
#pragma unroll
  for (int i = 0; i < 7; ++i) *(LAS u32x4*)(lds + r * RS + 16 * (c8 + 8 * i)) = xu[i];
  asm volatile("s_waitcnt vmcnt(0) lgkmcnt(0)\n\ts_barrier" ::: "memory");
  if (tid == 0) st_flag(P.ws + WS_FW + 128 * s, tag);
  STAMP(4);
#pragma unroll
  for (int i = 0; i < 7; ++i) {
    const int d8 = 8 * (wave * 7 + i);
    const u32 ad = (8 * (2 * q + (p >> 1)) + a) * RS + 2 * d8 + 8 * (p & 1);
    u32x4 v = __builtin_bit_cast(u32x4, tr_frag(lds, ad, ad + 4 * RS));
    LAS char* st = lds + 65536 + 1152 * (wave * 7 + i);
    *(LAS u32x4*)(st + 144 * (lane & 7) + 16 * (2 * q + ((lane >> 3) & 1))) = v;
    v = *(LAS u32x4*)(st + 144 * (lane >> 3) + 16 * (lane & 7));
    __builtin_amdgcn_raw_buffer_store_b128(v, rws, WS_UT + (d8 + (lane >> 3)) * TS + 128 * s + 16 * (lane & 7), 0, 16);
  }
  asm volatile("s_waitcnt vmcnt(0)\n\ts_barrier" ::: "memory");
  if (tid == 0) st_flag(P.ws + WS_FU + 128 * s, tag);
  STAMP(5);
  float gs = 0.f;
#pragma unroll
  for (int i = 0; i < 7; ++i)
#pragma unroll
    for (int j = 0; j < 4; ++j) {
      const u32 pr = pk_bf16(bf_lo(xu[i][j]) * bf_lo(gw[i][j]), bf_hi(xu[i][j]) * bf_hi(gw[i][j]));
      gs += bf_lo(pr) + bf_hi(pr);
    }
  gs += __shfl_xor(gs, 1);
  gs += __shfl_xor(gs, 2);
  gs += __shfl_xor(gs, 4);
  if (c8 == 0) {
    const float g = rbf(__builtin_amdgcn_rcpf(1.f + __expf(-rbf(gs))));
    __builtin_amdgcn_raw_buffer_store_b32(__float_as_uint(g), rws, WS_G + 4 * (64 * s + r), 0, 16);
  }
  asm volatile("s_waitcnt vmcnt(0)" ::: "memory");
  __syncthreads();
  if (tid == 0) st_flag(P.ws + WS_FG + 128 * s, tag);
  STAMP(1);
  STAMP_OUT;
  if (s == 0 && wave == 0) {
#pragma unroll 1
    for (int i = 0; i < 4; ++i) wait_flags(P.ws + WS_FG + 128 * (lane + 64 * i), tag);
    if (lane == 0) st_flag(P.ws + WS_EPOCH, tag);
  }
}

#define MF32(acc, a, b) acc = __builtin_amdgcn_mfma_f32_32x32x16_bf16(a, b, acc, 0, 0, 0)
#define MF16(acc, a, b) acc = __builtin_amdgcn_mfma_f32_16x16x32_bf16(a, b, acc, 0, 0, 0)
#define MFH(acc, a, b) acc = __builtin_amdgcn_mfma_f32_16x16x32_f16(a, b, acc, 0, 0, 0)
#define YOUNG_FIRST(n) do { if ((PRI >> (n)) & 1) asm volatile("s_cmp_lt_u32 %0, 4\n\ts_cbranch_scc1 1f\n\ts_setprio 1\n1:" :: "s"(wave) : "scc"); } while (0)
#define PRIO_OFF(n) do { if ((PRI >> (n)) & 1) __builtin_amdgcn_s_setprio(0); } while (0)
#define PRI 0
#define BARL do { SB0; asm volatile("s_waitcnt lgkmcnt(0)\n\ts_barrier" ::: "memory"); SB0; } while (0)
template <int N> DI void wait_vm() { asm volatile("s_waitcnt vmcnt(%0)" :: "n"(N) : "memory"); }
template <int N> struct IC { static constexpr int v = N; };

struct G1 {
  __amdgpu_buffer_rsrc_t rx, rg, ru;
  u32 vx[2], vw[2], wofs;
  u32 xa[3][4], wa[3][2];
};
struct G1Frag { s16x8 a[2], b[2]; };
template <int C, int I>
DI void g1_dma(const G1& d) {
  if constexpr (C < 7) {
    const u32 dst = (C % 3) * 49152u + (I >> 1) * 16384u + (I & 1) * 1024u + d.wofs;
    if constexpr (I < 2) dma16(dst, d.vx[I], d.rx, C * 128u);
    else dma16(dst, d.vw[I & 1], I < 4 ? d.rg : d.ru, C * 64u * 3584u);
  }
}
template <int C, int KS, int I>
DI void g1_rd(G1Frag& f, LAS char* lds, const G1& d) {
  if constexpr (C < 7) {
    if constexpr (I == 0) {
      f.b[0] = *(LAS s16x8*)(lds + d.xa[C % 3][KS]);
      f.b[1] = *(LAS s16x8*)(lds + d.xa[C % 3][KS] + 4096u);
    } else {
      f.a[I - 1] = tr_frag(lds, d.wa[C % 3][0] + I * 16384u + KS * 4096u, d.wa[C % 3][1] + I * 16384u + KS * 4096u);
    }
  }
}
#define PAD1 0
#define PAD2 0
#define PAD4 0
#define PADN(n) do { if ((n) > 0) { SB0; asm volatile("s_nop %0" :: "n"((n) > 16 ? 15 : (n) - 1)); if ((n) > 16) asm volatile("s_nop %0" :: "n"((n) > 16 ? (n) - 17 : 0)); } } while (0)
template <int S, class X>
DI void g1_step(f32x16 (&acc)[2][2], G1Frag (&f)[3], LAS char* lds, G1& d, X&& extra) {
  constexpr int NC = (S + 2) >> 2, NK = (S + 2) & 3;
  constexpr int X0 = S < 2 ? 12 + 3 * S : ((S - 2) & 3) < 3 ? 6 * (((S - 2) >> 2) + 3) + 2 * ((S - 2) & 3) : 99;
  constexpr int X2 = S < 2 ? X0 + 2 : 99;
  G1Frag& cur = f[S % 3];
  G1Frag& nxt = f[(S + 2) % 3];
  extra(IC<4 * S>{});
  SB0;
  MF32(acc[0][0], cur.a[0], cur.b[0]); SB0;
  g1_rd<NC, NK, 0>(nxt, lds, d);
  g1_dma<X0 / 6, X0 % 6>(d);
  extra(IC<4 * S + 1>{}); PADN(PAD1); SB0;
  MF32(acc[0][1], cur.a[0], cur.b[1]); SB0;
  g1_rd<NC, NK, 1>(nxt, lds, d);
  g1_dma<X2 / 6, X2 % 6>(d);
  extra(IC<4 * S + 2>{}); PADN(PAD2); SB0;
  MF32(acc[1][0], cur.a[1], cur.b[0]); SB0;
  g1_rd<NC, NK, 2>(nxt, lds, d);
  g1_dma<(X0 + 1) / 6, (X0 + 1) % 6>(d);
  extra(IC<4 * S + 3>{}); PADN(PAD1); SB0;
  MF32(acc[1][1], cur.a[1], cur.b[1]); SB0;
  extra(IC<1000 + S>{}); PADN(PAD4); SB0;
}
template <int C, class X>
DI void g1_chunk(f32x16 (&acc)[2][2], G1Frag (&f)[3], LAS char* lds, G1& d, X&& extra) {
  g1_step<4 * C>(acc, f, lds, d, extra);
  g1_step<4 * C + 1>(acc, f, lds, d, extra);
  g1_step<4 * C + 2>(acc, f, lds, d, extra);
  g1_step<4 * C + 3>(acc, f, lds, d, extra);
}

}

#define ROT 0
#define NWARM 0
#define LW_STEP 2
#define LW_EARLY 20
#define HPUB 6
#define LDWD(k) wdf[(k) >> 2][(k) & 3] = __builtin_bit_cast(s16x8, __builtin_amdgcn_raw_buffer_load_b128( \
    rws, lane * 16, WS_WD + ((cb * 56 + 7 * wave) * 4 + (k)) * 1024, 16))
#define FHP 6
#define G2_PRE0 13
#define EPI_OFF 2
#define HAUX 16
#define G2_PRE1 G2_PRE0
#define FAP 21
#define FAC 26
#define TMODE 0
#define FHE 0
#define FHC 5
#define G2_PREC 6
#define G2_LA 7
#define D0POS 2
#define D1E 0
#define KPRE 1
#define D1KS 3
#define CP_EPI 4
#define APUB 3
#define G2_PRE 14

__global__ __launch_bounds__(512, 2) void gated_mix_kernel(Params P) {
  __shared__ __attribute__((aligned(16))) char smem[163840];
  LAS char* lds = (LAS char*)smem;
  const int tid = threadIdx.x, lane = tid & 63;
  const int wave = __builtin_amdgcn_readfirstlane(tid >> 6);
  const int bid = blockIdx.x;
  STAMP_DECL;
  STAMP(0);
  if (bid < 32) {
    helper_block(P, lds, bid, tid, lane, wave);
    return;
  }
  const int xcd = bid & 7, slot = (bid >> 3) - 4;
  const int tb = 4 * (xcd >> 1) + slot / 7, fb = 7 * (xcd & 1) + slot % 7;
  const int mid = tb * 14 + fb;
  const int rb = 2 * tb + (xcd & 1), cb = slot % 7;
  const int ch = wave >> 2, d0 = 2 * mid;
  const int q = lane >> 4, r16 = lane & 15;

  const u32 tbl = LD_TB + TB_STRIDE * wave;
  {
    u64 po = mid * 1024 + wave * 128 + (lane & 7) * 16;
    u32 m0v = tbl + 4096;
    asm volatile("" : "+v"(po), "+s"(m0v));
    SB0;
    u32 lo, hi;
#define LO(p) "s"((u32)(u64)(p))
#define HI(p) "s"((u32)((u64)(p) >> 32))
    asm volatile("v_mov_b32 %0, %2\n\tv_mov_b32 %1, %3\n\ts_lshl_b64 exec, exec, 8\n\t"
                 "v_mov_b32 %0, %4\n\tv_mov_b32 %1, %5\n\ts_lshl_b64 exec, exec, 8\n\t"
                 "v_mov_b32 %0, %6\n\tv_mov_b32 %1, %7\n\ts_lshl_b64 exec, exec, 8\n\t"
                 "v_mov_b32 %0, %8\n\tv_mov_b32 %1, %9\n\ts_lshl_b64 exec, exec, 8\n\t"
                 "v_mov_b32 %0, %10\n\tv_mov_b32 %1, %11\n\ts_lshl_b64 exec, exec, 8\n\t"
                 "v_mov_b32 %0, %12\n\tv_mov_b32 %1, %13\n\ts_mov_b64 exec, -1"
                 : "=&v"(lo), "=&v"(hi)
                 : LO(P.are), HI(P.are), LO(P.aim), HI(P.aim), LO(P.bre), HI(P.bre), LO(P.bim), HI(P.bim), LO(P.cre), HI(P.cre),
                   LO(P.cim), HI(P.cim) : "scc");
    asm volatile("s_mov_b32 m0, %0\n\ts_nop 0\n\tglobal_load_lds_dwordx4 %1, off"
                 :: "s"(m0v), "v"((((u64)hi << 32) | lo) + po) : "memory");
  }
  u32 wm0;
  if constexpr (NWARM > 0) {
    const int xj = 5 * wave + (lane - 16 < 5 ? lane - 16 : 0), xr = 19 * (slot % 7) + (xj >> 1);
    u32 ow = (16 * wave + 4 * (lane >> 2) + slot / 7) * 3584 + fb * 256 + (lane & 1) * 128;
    u32 ox = (128 * tb + (xr < 128 ? xr : 127)) * 896 + 128 * (xj & 1);
    asm volatile("" : "+v"(ow), "+v"(ox));
    const u32 o0 = lane < 16 ? ow : ox;
    u32 lo, hi;
    asm volatile("v_mov_b32 %0, %2\n\tv_mov_b32 %1, %3\n\ts_mov_b64 exec, 0x3333\n\tv_mov_b32 %0, %4\n\tv_mov_b32 %1, %5\n\t"
                 "s_mov_b64 exec, 0xcccc\n\tv_mov_b32 %0, %6\n\tv_mov_b32 %1, %7\n\ts_mov_b64 exec, -1"
                 : "=&v"(lo), "=&v"(hi) : LO(P.u), HI(P.u), LO(P.wg), HI(P.wg), LO(P.wu), HI(P.wu));
    asm volatile("s_mov_b64 exec, 0x1fffff\n\tglobal_load_dword %0, %1, off\n\ts_mov_b64 exec, -1"
                 : "=v"(wm0) : "v"((((u64)hi << 32) | lo) + o0) : "memory");
  }
  u64 tag = *(const CAS u64*)(P.ws + WS_EPOCH);
  const u32 p_ldt = *(const CAS u32*)(P.ldt + d0), p_dsk = *(const CAS u32*)(P.dsk + d0);
  SB0;
  G1 d;
  d.rx = mkrs(P.u + (size_t)tb * 128 * D_, 128 * D_ * 2);
  d.rg = mkrs(P.wg + fb * 128, (D_ * F_ - fb * 128) * 2);
  d.ru = mkrs(P.wu + fb * 128, (D_ * F_ - fb * 128) * 2);
  d.wofs = 2048u * wave;
#pragma unroll
  for (int i = 0; i < 2; ++i) {
    d.vx[i] = (16 * wave + 8 * i + (lane >> 3)) * 896 + 16 * ((lane & 7) ^ ((4 * i + q) & 7));
    d.vw[i] = (8 * wave + 4 * i + q) * 3584 + 16 * (r16 ^ ((q << 2) | ((2 * wave + i) & 3)));
  }
  if constexpr (D0POS == 0) {
  g1_dma<0, 0>(d); g1_dma<0, 2>(d); g1_dma<0, 4>(d); g1_dma<0, 1>(d); g1_dma<0, 3>(d); g1_dma<0, 5>(d);
  if constexpr (D1E > 0) g1_dma<1, 0>(d);
  if constexpr (D1E > 1) g1_dma<1, 1>(d);
  }
  auto rws = mkrs(P.ws, WS_END);
  STAMP(14);
  const int wf = wave & 3, wt = wave >> 2, hh = lane >> 5;
#pragma unroll
  for (int st = 0; st < 1; ++st) {
#pragma unroll
    for (int ks = 0; ks < 4; ++ks) {
      d.xa[st][ks] = 49152 * st + 128 * (64 * wt + (lane & 31)) + 16 * ((2 * ks + hh) ^ ((lane >> 1) & 7));
      asm volatile("" : "+v"(d.xa[st][ks]));
    }
    const int blk = q & 1, a = r16 >> 2, p = lane & 3;
#pragma unroll
    for (int t = 0; t < 2; ++t) {
      d.wa[st][t] = 49152 * st + 256 * (8 * hh + 4 * t + a) + 16 * ((4 * wf + 2 * blk + (p >> 1)) ^ ((a << 2) | ((2 * hh + t) & 3))) + 8 * (p & 1);
      asm volatile("" : "+v"(d.wa[st][t]));
    }
  }
  const int tl = tid & 255;
  u32 kofs[8], ra[7], wr[5];
  {
    const int hi = 4 * (r16 >> 2), lo = 4 * (r16 & 3);
    const u32 row[7] = {14 + (u32)(r16 >> 2), (0xd847u >> lo) & 15, (0xa317u >> hi) & 15, (0xb967u >> lo) & 15, (0x7717u >> hi) & 15,
                        (0xc022u >> hi) & 15, 5};
#pragma unroll
    for (int i = 0; i < 7; ++i) {
      ra[i] = tbl + 288 * row[i] + 16 * q;
      asm volatile("" : "+v"(ra[i]));
    }
#pragma unroll
    for (int i = 0; i < 5; ++i) {
      wr[i] = tbl + 4 * lane + 4 * 288 * i;
      asm volatile("" : "+v"(wr[i]));
    }
  }
  const float dt = rbf(dexpf(ch ? bf_hi(p_ldt) : bf_lo(p_ldt)));
#define KPUT(row, v) *(LAS u32*)(lds + wr[(row) >> 2] + 288 * ((row) & 3)) = (v)
  SB0;
  if constexpr (D0POS == 1) {
  g1_dma<0, 0>(d); g1_dma<0, 2>(d); g1_dma<0, 4>(d); g1_dma<0, 1>(d); g1_dma<0, 3>(d); g1_dma<0, 5>(d);
  if constexpr (D1E > 0) g1_dma<1, 0>(d);
  if constexpr (D1E > 1) g1_dma<1, 1>(d);
  }
  if constexpr (D0POS == 2) {
    if (wave < 4) {
#pragma unroll
      for (int hf2 = 0; hf2 < 2; ++hf2) {
        dma16(d.wofs + 8192 * hf2, d.vx[0], d.rx, hf2 * 64 * 896);
        dma16(16384 + d.wofs + 8192 * hf2, d.vw[0], d.rg, hf2 * 32 * 3584);
        dma16(32768 + d.wofs + 8192 * hf2, d.vw[0], d.ru, hf2 * 32 * 3584);
        dma16(1024 + d.wofs + 8192 * hf2, d.vx[1], d.rx, hf2 * 64 * 896);
        dma16(1024 + 16384 + d.wofs + 8192 * hf2, d.vw[1], d.rg, hf2 * 32 * 3584);
        dma16(1024 + 32768 + d.wofs + 8192 * hf2, d.vw[1], d.ru, hf2 * 32 * 3584);
      }
      wait_vm<NWARM + 12>();
    } else {
      wait_vm<NWARM>();
    }
  }
  STAMP(15);
  SB0;
  if constexpr (D0POS != 2) wait_vm<NWARM + 6 + D1E>();
  u32 prm[6];
#pragma unroll
  for (int i = 0; i < 6; ++i) prm[i] = *(LAS u16*)(lds + tbl + 4096 + 128 * i + 2 * lane);
  SB0;
  STAMP(16);
  f32x4 kq[3] = {};
  u32x4 kx[4][7], kt0, ktc, kfa, kt1;
  float wunv, klv;
  auto kblock = [&](auto I) {
    constexpr int B = decltype(I)::v, ks = B / 5, g = B % 5;
    const u32x4(&c)[7] = kx[ks];
    if constexpr (g == 0) kt0 = cmulh4(c[2], c[3]);
    if constexpr (g == 1) ktc = cmulh4(c[4], c[3]);
    if constexpr (g == 2) kfa = cmulh4(c[0], c[1]);
    if constexpr (g == 3) { kt1 = cmulh4(kt0, c[6]); SB0; MFH(kq[0], H8(kfa), H8(kt0)); }
    if constexpr (g == 4) {
      const u32x4 t2 = cmulh4(ktc, c[5]);
      SB0;
      MFH(kq[1], H8(kfa), H8(kt1));
      asm volatile("s_nop 0");
      SB0;
      MFH(kq[2], H8(kfa), H8(t2));
    }
    if constexpr (B == 0) klv = dpp_add<0x111, 0xf>(klv);
    if constexpr (B == 1) klv = dpp_add<0x112, 0xf>(klv);
    if constexpr (B == 2) klv = dpp_add<0x114, 0xf>(klv);
    if constexpr (B == 5) klv = dpp_add<0x118, 0xf>(klv);
    if constexpr (B == 6) klv = dpp_add<0x142, 0xa>(klv);
    if constexpr (B == 7) klv = dpp_add<0x143, 0xc>(klv);
    SB0;
  };
  {
    const float a2 = (dt * bf_lo(prm[0])) * 1.4426950408889634f, b2 = (dt * bf_lo(prm[1])) * 0.15915494309189535f;
    const float br = dt * bf_lo(prm[2]), bi = dt * bf_lo(prm[3]), cr = bf_lo(prm[4]), ci = bf_lo(prm[5]);
    const f32x2 v = {cr * br - ci * bi, -(cr * bi + ci * br)};
    const float f1 = __builtin_amdgcn_fractf(-b2), f16 = __builtin_amdgcn_fractf(-16.f * b2), f256 = __builtin_amdgcn_fractf(256.f * b2);
    u32 wm = __builtin_bit_cast(u32, __builtin_fmaxf(__builtin_fabsf(v.x), __builtin_fabsf(v.y)));
    const float e1 = __builtin_amdgcn_exp2f(a2);
    SBV;
    wm = dpp_umax<0x111, 0xf>(wm);
    const float e16 = __builtin_amdgcn_exp2f(16.f * a2), e256 = __builtin_amdgcn_exp2f(256.f * a2);
    SBV;
    wm = dpp_umax<0x112, 0xf>(wm);
    const float c1 = __builtin_amdgcn_cosf(f1), s1 = __builtin_amdgcn_sinf(f1);
    SBV;
    wm = dpp_umax<0x114, 0xf>(wm);
    const float c16 = __builtin_amdgcn_cosf(f16), s16 = __builtin_amdgcn_sinf(f16);
    SBV;
    wm = dpp_umax<0x118, 0xf>(wm);
    const float c256 = __builtin_amdgcn_cosf(f256), s256 = __builtin_amdgcn_sinf(f256);
    SBV;
    wm = dpp_umax<0x142, 0xa>(wm);
    const f32x2 z1 = f32x2{c1, s1} * e1, z16 = f32x2{c16, s16} * e16, z256 = f32x2{c256, s256} * e256;
    SBV;
    wm = dpp_umax<0x143, 0xc>(wm);
    f32x2 l1[4], l2[4], l3[4], l4[4];
    cmul4(l1, {z1, z256, v, z16}, {z1, z256, z16, z16});
    SBV;
    KPUT(0, cvth_conj(z1)); KPUT(1, cvth(z256)); KPUT(7, 0x3c00u);
    u32 we = __builtin_amdgcn_readlane(wm, 63) >> 23;
    SBV;
    cmul4(l2, {l1[0], l1[1], l1[2], l1[3]}, {l1[0], l1[1], z16, l1[3]});
    SBV;
    KPUT(2, cvth_conj(l1[0])); KPUT(3, cvth(l1[1]));
    we = we < 1 ? 1 : we > 253 ? 253 : we;
    const float wsc = __builtin_bit_cast(float, (254 - we) << 23), wun = __builtin_bit_cast(float, we << 23);
    SBV;
    cmul4(l3, {l2[0], l2[1], l2[2], l2[3]}, {l2[0], l2[1], z16, l2[3]});
    SBV;
    KPUT(4, cvth(l2[0])); KPUT(5, cvth(l2[1])); KPUT(6, cvth_conj(l2[3]));
    KPUT(14, cvth(v * wsc)); KPUT(15, cvth(l1[2] * wsc));
    SBV;
    cmul4(l4, {l1[0], l1[1], l3[0], l3[3]}, {z1, z256, l2[0], l2[3]});
    SBV;
    KPUT(8, cvth(l3[0])); KPUT(9, cvth_conj(l3[3]));
    KPUT(16, cvth(l2[2] * wsc)); KPUT(17, cvth(l3[2] * wsc));
    float kl = v.x * l3[1].x + v.y * l3[1].y;
    SBV;
    KPUT(10, cvth(l4[1])); KPUT(11, cvth_conj(l4[3])); KPUT(12, cvth_conj(l4[0])); KPUT(13, cvth(l4[2]));
    STAMP(17);
    SB0;
    if constexpr (D1E < 1) g1_dma<1, 0>(d);
    if constexpr (D1E < 2) g1_dma<1, 1>(d);
    g1_dma<1, 2>(d); g1_dma<1, 3>(d); g1_dma<1, 4>(d); g1_dma<1, 5>(d);
    STAMP(6);
    wunv = wun;
    klv = kl;
#pragma unroll
    for (int ks = 0; ks < 4; ++ks)
#pragma unroll
      for (int j = 0; j < 7; ++j) kx[ks][j] = *(LAS u32x4*)(lds + ra[j] + 64 * ks);
    SB0;
    kblock(IC<0>{}); kblock(IC<1>{}); kblock(IC<2>{}); kblock(IC<3>{}); kblock(IC<4>{});
    if constexpr (KPRE > 1) { kblock(IC<5>{}); kblock(IC<6>{}); kblock(IC<7>{}); kblock(IC<8>{}); kblock(IC<9>{}); }
    STAMP(18);
  }
  asm volatile("" : "+s"(tag));
  tag += 1;
  u64 tagv = tag;
  asm volatile("" : "+v"(tagv));
  STAMP(19);
  if constexpr (NWARM > 0) asm volatile("s_waitcnt vmcnt(6)" : "+v"(wm0) :: "memory");
  else wait_vm<6>();
  BARL;
  G1Frag fr[3];
  g1_rd<0, 0, 0>(fr[0], lds, d); g1_rd<0, 0, 1>(fr[0], lds, d); g1_rd<0, 0, 2>(fr[0], lds, d);
  g1_rd<0, 1, 0>(fr[1], lds, d); g1_rd<0, 1, 1>(fr[1], lds, d); g1_rd<0, 1, 2>(fr[1], lds, d);
  STAMP(1);

  s16x8 wdf[7][4];
  u32x4 urow;
  const char* fgp = P.ws + WS_FU + 128 * (lane & 31);
  u64 fg;
  f32x16 acc[2][2] = {};
  LAS char* cp = lds + LD_CP + CP_STRIDE * ch;
  LAS char* upl = lds + LD_UP + UP_STRIDE * ch;
  u32x4 klo, khi;
  const int cj = (wave + ch) & 3, wj = wave & 3;
  u32 ab[2], bb[2], exa, exs, fho, exw, exr, exo, uur, ato;
  u32 aa[7], cb0;
  auto kcopy = [&](auto I) {
    constexpr int sh = decltype(I)::v, o = (7 - sh) >> 1;
    const u32 dd[8] = {klo[0], klo[1], klo[2], klo[3], khi[0], khi[1], khi[2], khi[3]};
    *(LAS u32x4*)(cp + (cb0 ^ (16 * sh))) =
        (sh & 1) ? u32x4{dd[o], dd[o + 1], dd[o + 2], dd[o + 3]} : u32x4{aa[o], aa[o + 1], aa[o + 2], aa[o + 3]};
  };
  {
    u64 fu;
    f32x4 kt[4];
    u32 kp4[4], kro = LD_KP + KR_STRIDE * wave + 64 * r16 + 16 * (q ^ ((r16 >> 1) & 3));
    asm volatile("" : "+v"(kro));
    float kg[8];
    u32 zo;
    const char* fup = P.ws + WS_FW + 128 * (((7 * wave) >> 1) + ((lane & 3) < 3 ? (lane & 3) : ((7 * wave + 6) >> 1) - ((7 * wave) >> 1)));

    u32x4 kt2;
    u32 tv0, tv1;
#define PIN1(a) asm volatile("" : "+v"(a))
#define PIN2(a, b) asm volatile("" : "+v"(a), "+v"(b))
#define PIN3(a, b, c) asm volatile("" : "+v"(a), "+v"(b), "+v"(c))
    auto task = [&](auto I) {
      constexpr int N0 = decltype(I)::v;
      if constexpr (N0 == 1 || N0 == 3 || N0 == 17 || N0 == 19) {
        constexpr int st = N0 < 8 ? 1 : 2, h = (N0 >> 1) & 1;
        d.xa[st][2 * h] = d.xa[0][2 * h] + 49152 * st; d.xa[st][2 * h + 1] = d.xa[0][2 * h + 1] + 49152 * st;
        d.wa[st][h] = d.wa[0][h] + 49152 * st;
        PIN3(d.xa[st][2 * h], d.xa[st][2 * h + 1], d.wa[st][h]);
      }
      constexpr int N = N0 - (N0 > 1) - (N0 > 3) - (N0 > 17) - (N0 > 19);
      constexpr bool own = !(N0 == 1 || N0 == 3 || N0 == 17 || N0 == 19);
      constexpr int NK = 10 * (4 - KPRE);
      if constexpr (own && N < NK) {
        constexpr int ks = KPRE + N / 10, g = N % 10;
        const u32x4(&c)[7] = kx[ks];
        if constexpr (g == 0) kt0 = mulh4(c[2], c[3]);
        if constexpr (g == 1) kt0 = fmah4(kt0, c[2], c[3]);
        if constexpr (g == 2) kfa = mulh4(c[0], c[1]);
        if constexpr (g == 3) kfa = fmah4(kfa, c[0], c[1]);
        if constexpr (g == 4) ktc = mulh4(c[4], c[3]);
        if constexpr (g == 5) { ktc = fmah4(ktc, c[4], c[3]); SB0; MFH(kq[0], H8(kfa), H8(kt0)); }
        if constexpr (g == 6) kt1 = mulh4(kt0, c[6]);
        if constexpr (g == 7) kt1 = fmah4(kt1, kt0, c[6]);
        if constexpr (g == 8) kt2 = mulh4(ktc, c[5]);
        if constexpr (g == 9) { kt2 = fmah4(kt2, ktc, c[5]); SB0; MFH(kq[1], H8(kfa), H8(kt1)); }
        if constexpr (g == 0 && N > 0) { SB0; MFH(kq[2], H8(kfa), H8(kt2)); }
        if constexpr (KPRE == 1 && N == 1) klv = dpp_add<0x118, 0xf>(klv);
        if constexpr (KPRE == 1 && N == 3) klv = dpp_add<0x142, 0xa>(klv);
        if constexpr (KPRE == 1 && N == 7) klv = dpp_add<0x143, 0xc>(klv);
      }
      constexpr int M = own ? N - NK : -1;
      if constexpr (M == 0) MFH(kq[2], H8(kfa), H8(kt2));
      if constexpr (M >= 1 && M <= 4) {
        constexpr int i = (M - 1) >> 1;
        const int rho = 2 * cj + i, sft = rho >= 5;
        if constexpr (M & 1) {
          tv0 = 19 - 4 * rho - q;
          bb[i] = LD_UP + UP_STRIDE * ch + 512 + 64 * rho - 512 * sft + 16 * (r16 + q);
          tv1 = 16 * ((lane ^ tv0) & 7);
          PIN3(tv0, tv1, bb[i]);
        } else {
          ab[i] = LD_CP + CP_STRIDE * ch + 4096 * sft + 128 * (2 * (lane & 8) + tv0) + tv1;
          PIN1(ab[i]);
        }
      }
      if constexpr (M >= 5 && M <= 7) {
        kq[M - 5] = mul4(kq[M - 5], wunv);
        if constexpr (M == 6) { if (lane == 63) kq[1][3] = klv; }
      }
      if constexpr (M == 8) { tv0 = (255 - tl) * 0x80008u; tv1 = tl == 0 ? 0x40004u : 0u; PIN2(tv0, tv1); }
      if constexpr (M >= 9 && M <= 12) {
        constexpr int i = M - 9;
        constexpr u32 c0[4] = {2564u << 16 | 3332, 4u << 16 | 3076, 2560u << 16 | 3328, 3072}, c1[4] = {2564u << 16 | 8, 0x40004, 2560u << 16 | 4, 0};
        constexpr u32 c2[4] = {0x80008, 0x40004, 4, 0}, c3[4] = {0x80008, 8, 0, 0};
        u32 lo = wf == 3 ? c1[i] : wf == 2 ? c2[i] : c3[i], hi = wf == 3 ? c0[i] : lo;
        asm volatile("" : "+s"(lo), "+s"(hi));
        kp4[i] = (lane < 32 ? lo : hi) + tv0;
        if constexpr (i < 2) kp4[i] -= tv1;
        PIN1(kp4[i]);
      }
      if constexpr (M == 13) { kp4[1] += wf < 2 ? (~tl & 1) << 19 : 0u; PIN1(kp4[1]); }
      if constexpr (M >= 14 && M <= 17) {
        constexpr int i = M - 14;
        const u32 t = kp4[i] ^ ((kp4[i] >> 3) & 0x300030u);
        kofs[2 * i] = LD_KR + KR_STRIDE * ch + (t & 0xffffu);
        kofs[2 * i + 1] = LD_KR + KR_STRIDE * ch + (t >> 16);
        PIN2(kofs[2 * i], kofs[2 * i + 1]);
      }
      if constexpr (M == 22) { tv0 = 7 * wave + ((lane & 7) < 6 ? (lane & 7) : 6); PIN1(tv0); }
      if constexpr (M == 23) { fho = WS_FH + 128 * tb * 14 + 64 * (xcd & 1) + 32 * (tv0 & ~3u) + 16 * (tv0 & 3); PIN1(fho); }
      if constexpr (M == 18) {
        asm volatile("s_cmp_lt_u32 %2, 3\n\ts_cselect_b32 %0, %3, %4\n\ts_cselect_b32 %1, %5, %6" : "=&s"(exa), "=&s"(exs)
                     : "s"(wj), "s"(LD_EX + 1024 * (3 * ch + wj)), "s"(LD_EXO + 1024 * ch), "s"(6144), "s"(2048) : "scc");
      }
      if constexpr (M == 19) { exw = exa + ((16 * lane) ^ (8 * (lane & 16))); PIN1(exw); }
      if constexpr (M == 20) { tv0 = 16 * (lane >> 2) + 8 * (lane & 1); tv1 = (lane >> 1) & 1; PIN2(tv0, tv1); }
      if constexpr (M == 21) { tv0 = (tv0 ^ (128 * tv1)) + 256 * tv1; exr = LD_EX + 6144 * wave + tv0; exo = LD_EXO + 2048 * wave + tv0; PIN2(exr, exo); }
      if constexpr (M == 24) { fho = lane < 8 ? fho : WS_FG + 128 * rb; PIN1(fho); }
      if constexpr (M == 25) {
        uur = LD_UP + 512 + 512 * wave + 4 * lane;
        ato = WS_AT + mid * AS + 2048 * wave + 16 * lane;
        PIN2(uur, ato);
      }
      if constexpr (M == 27) { zo = LD_CP + CP_STRIDE * ch + 16 * tl + (tl >= 152 ? 275 * 128 - 16 * 152 : 0); PIN1(zo); }
      if constexpr (M == 28) { zo += tl >= 160 ? (LD_UP - LD_CP) - (CP_STRIDE - UP_STRIDE) * ch - 275 * 128 - 16 * 8 : 0; PIN1(zo); }
      if constexpr (M == 29) { zo += tl >= 192 ? 4608 - 16 * 32 : 0; PIN1(zo); }
      if constexpr (M == 30) { cb0 = 128 * (tl + 19) + 16 * ((tl + 19) & 7); PIN1(cb0); }
    };
    auto extra = [&](auto I) {
      constexpr int X = decltype(I)::v, S = X >= 1000 ? 99 : X >> 2, J = X & 3;
      constexpr int SLW = 17;
      if constexpr (J == 0 && (S & 3) == 2 && S < 26) {
        constexpr int nlw = LW_STEP * (S - SLW) < LW_EARLY ? LW_STEP * (S - SLW) : LW_EARLY;
        constexpr int N = S == 14 ? 6 + 1 : S == 18 ? 63 : S == 22 ? nlw : 6;
        asm volatile("s_waitcnt vmcnt(%0) lgkmcnt(0)\n\ts_barrier" :: "n"(N) : "memory");
      }
      if constexpr (TMODE == 0) {
        if constexpr (X >= 1000) task(IC<4 * (X - 1000) + 3>{});
        else if constexpr (J >= 1) task(IC<4 * S + J - 1>{});
      } else if constexpr (TMODE == 1) {
        if constexpr (X >= 1000) { task(IC<4 * (X - 1000) + 2>{}); SB0; task(IC<4 * (X - 1000) + 3>{}); }
        else if constexpr (J == 2) { task(IC<4 * S>{}); SB0; task(IC<4 * S + 1>{}); }
      } else {
        if constexpr (X >= 1000) { task(IC<4 * (X - 1000) + 1>{}); SB0; task(IC<4 * (X - 1000) + 2>{}); SB0; task(IC<4 * (X - 1000) + 3>{}); }
        else if constexpr (J == 2) task(IC<4 * S>{});
      }
      if constexpr (S == 19 && J >= 1) {
        constexpr u32 so[3] = {0, 1024, 2560};
        *(LAS f32x4*)(lds + kro + so[J - 1]) = kq[J - 1];
      }
      if constexpr ((S == 20 || S == 25) && J == 0) asm volatile("s_waitcnt lgkmcnt(0)\n\ts_barrier" ::: "memory");
      if constexpr (S == 20 && J >= 1 && J <= 2) {
#pragma unroll
        for (int i = 2 * J - 2; i < 2 * J; ++i) kt[i] = *(LAS f32x4*)(lds + LD_KP + KR_STRIDE * (4 * ch + i) + 16 * (tl < 224 ? tl : 0));
      }
      if constexpr (S == 21 && J == 1) { kt[0] = add4(kt[0], kt[1]); kt[2] = add4(kt[2], kt[3]); }
      if constexpr (S == 21 && J == 2) kt[0] = add4(kt[0], kt[2]);
      if constexpr (S == 21 && J == 3) { if (tl < 224) *(LAS f32x4*)(lds + LD_KR + KR_STRIDE * ch + 16 * tl) = kt[0]; }
      if constexpr (S == 22 && J >= 1) {
#pragma unroll
        for (int j = J == 1 ? 0 : 3 * J - 4; j < (J == 1 ? 2 : 3 * J - 1); ++j) kg[j] = *(LAS float*)(lds + kofs[j]);
      }
      if constexpr (S == 24 && J == 1)
        *(LAS u32x4*)(lds + LD_RK + 4096 * ch + 16 * tl) =
            u32x4{pk_bf16(kg[0], kg[1]), pk_bf16(kg[2], kg[3]), pk_bf16(kg[4], kg[5]), pk_bf16(kg[6], kg[7])};
      if constexpr (S == 13 && J == 1) asm volatile("global_load_dwordx2 %0, %1, off sc1" : "=v"(fu) : "v"(fup) : "memory");
      if constexpr (S == SLW && J == 0) {
        asm volatile("s_waitcnt vmcnt(6)" : "+v"(fu) :: "memory");
        if (__builtin_expect(!all_eq(fu, tag), 0)) { FALLBACK(0); wait_flags(fup, tag); }
        asm volatile("" ::: "memory");
      }
      if constexpr (S == 26 && J == 2) fg = ld_flag(fgp);
      if constexpr (S >= SLW && J >= 1 && J <= LW_STEP) {
        constexpr int k = LW_STEP * (S - SLW) + J - 1;
        if constexpr (k < LW_EARLY) LDWD(k);
      }
      if constexpr (S == 25 && J == 1) {
        klo = *(LAS u32x4*)(lds + LD_RK + 4096 * ch + 16 * (255 - tl));
        khi = *(LAS u32x4*)(lds + LD_RK + 4096 * ch + 16 * (tl ? 256 - tl : 0));
      }
      if constexpr (S == 23 && J == 2) {
        if (tl < 196) *(LAS u32x4*)(lds + zo) = u32x4{0, 0, 0, 0};
      }
      if constexpr (S == 26 && J == 0) {
        if (tl == 0) khi = u32x4{0, 0, 0, 0};
        const u32 dd[8] = {klo[0], klo[1], klo[2], klo[3], khi[0], khi[1], khi[2], khi[3]};
#pragma unroll
        for (int k = 0; k < 7; ++k) aa[k] = __builtin_amdgcn_alignbit(dd[k + 1], dd[k], 16);
      }
      if constexpr ((S >= 26 && S < 28 && J >= 1) || X == 1026 || X == 1027) {
        constexpr int sl = X == 1026 ? 3 : X == 1027 ? 7 : 4 * (S - 26) + J - 1;
        if constexpr (CP_EPI == 0) kcopy(IC<sl>{});
        else if constexpr (CP_EPI == 4 && (sl & 1) == 0) kcopy(IC<sl / 2>{});
      }
    };
    g1_chunk<0>(acc, fr, lds, d, extra);
    STAMP(2);
    g1_chunk<1>(acc, fr, lds, d, extra);
    STAMP(27);
    g1_chunk<2>(acc, fr, lds, d, extra);
    STAMP(28);
    g1_chunk<3>(acc, fr, lds, d, extra);
    STAMP(3);
    g1_chunk<4>(acc, fr, lds, d, extra);
    STAMP(29);
    g1_chunk<5>(acc, fr, lds, d, extra);
    STAMP(20);
    YOUNG_FIRST(1);
    g1_chunk<6>(acc, fr, lds, d, extra);
  }
  asm volatile("s_nop 7\n\ts_nop 7");
  STAMP(4);
  {
    const u32 hv = WS_H + ((u32)(tb * 8 + 4 * wt + (q & 1)) * 56 + fb * 4 + wf) * 1024 + hh * 256 + r16 * 16;
#pragma unroll
    for (int gi = 0; gi < 4; ++gi) {
      const int tt = gi >> 1, s = gi & 1;
      if (gi == EPI_OFF) PRIO_OFF(1);
      f32x2 g2[4], u2[4], e[4], gv[4], hv2[4];
#pragma unroll
      for (int j = 0; j < 4; ++j) {
        g2[j] = f32x2{acc[0][tt][8 * s + 2 * j], acc[0][tt][8 * s + 2 * j + 1]};
        u2[j] = f32x2{acc[1][tt][8 * s + 2 * j], acc[1][tt][8 * s + 2 * j + 1]};
      }
      pk4_mul_s(e, g2, f32x2{-1.4426950408889634f, -1.4426950408889634f});
      SBV;
      pk4_mul(gv, g2, u2);
      SB0;
      if (CP_EPI == 8) {
        if (gi == 0) kcopy(IC<4>{});
        if (gi == 1) kcopy(IC<5>{});
        if (gi == 2) kcopy(IC<6>{});
        if (gi == 3) kcopy(IC<7>{});
      }
      SB0;
#pragma unroll
      for (int j = 0; j < 4; ++j) e[j] = f32x2{__builtin_amdgcn_exp2f(e[j].x), __builtin_amdgcn_exp2f(e[j].y)};
      SBV;
      pk4_add1(e);
      SB0;
      if (CP_EPI >= 4) {
        if (gi == 0) kcopy(IC<8 - CP_EPI>{});
        if (gi == 1) kcopy(IC<9 - CP_EPI>{});
        if (gi == 2) kcopy(IC<10 - CP_EPI>{});
        if (gi == 3) kcopy(IC<11 - CP_EPI>{});
      }
      SB0;
#pragma unroll
      for (int j = 0; j < 4; ++j) e[j] = f32x2{__builtin_amdgcn_rcpf(e[j].x), __builtin_amdgcn_rcpf(e[j].y)};
      SBV;
      asm volatile("s_nop 0");
      pk4_mul(hv2, gv, e);
      SBV;
      const u32x4 o = {pk_bf16(hv2[0]), pk_bf16(hv2[1]), pk_bf16(hv2[2]), pk_bf16(hv2[3])};
      SBV;
      if (gi == 0) {
        if (__builtin_expect(!all_eq(fg, tag), 0)) { FALLBACK(1); wait_flags(fgp, tag); }
        asm volatile("" ::: "memory");
        urow = __builtin_amdgcn_raw_buffer_load_b128(rws, 16 * tl, WS_UT + (d0 + ch) * TS, 16);
      }
      __builtin_amdgcn_raw_buffer_store_b128(o, rws, hv + tt * 2 * 56 * 1024 + s * 512, 0, 16);
    }
  }
  *(LAS u32x4*)(upl + 512 + 16 * tl) = urow;
  STAMP(5);
  YOUNG_FIRST(2);
  BARL;
  STAMP(11);
  u64 fh;
  u32 ppa[4], avo, gv;
    s16x8 hf[28];
    const u32 hs = WS_H + ((u32)rb * 4 * 56 + 7 * wave) * 1024;
    u32 hst[4];
#pragma unroll
    for (int i = 0; i < 4; ++i) hst[i] = hs + ((i + ROT * cb) & 3) * 56 * 1024;
#define LDH(i) hf[i] = __builtin_bit_cast(s16x8, __builtin_amdgcn_raw_buffer_load_b128(rws, lane * 16 + ((i) % 7 & 3) * 1024, hst[(i) / 7] + ((i) % 7 >> 2) * 4096, HAUX))
  {
    f32x4 ya[8] = {};
#define RDA(i, e) *(LAS s16x8*)(lds + ab[i] + 4096 * (e))
#define RDB(i, p) *(LAS s16x8*)(lds + bb[i] + 512 * (p))
#define LWC(e) (LW_EARLY + ((28 - LW_EARLY) * (HPUB - (e)) + HPUB - 1) / HPUB)
    s16x8 a0[8], b0[8], a1[8], b1[8];
    bool hok = false;
#pragma unroll
    for (int e = 7; e >= 0; --e) { a0[e] = RDA(0, e); b0[7 - e] = RDB(0, 7 - e); }
    SB0;
#pragma unroll
    for (int e = 7; e >= 0; --e) {
#pragma unroll
      for (int jt = e; jt < 8; ++jt) MF16(ya[jt], a0[e], b0[jt - e]);
      SB0;
      if (e < 3) { a1[2 - e] = RDA(1, 2 - e); b1[2 - e] = RDB(1, 2 - e); }
      if (e < HPUB) {
#pragma unroll
        for (int k = LWC(e + 1); k < LWC(e); ++k) LDWD(k);
      }
      if (e == FHE) fh = __builtin_bit_cast(u64, __builtin_amdgcn_raw_buffer_load_b64(rws, fho, 0, 16));
      if (e == 2) {
#pragma unroll
    for (int i = 0; i < 4; ++i) {
      ppa[i] = 4096 * wave + 256 * r16 + 16 * ((4 * i + q) ^ r16) + 49152;
      asm volatile("" : "+v"(ppa[i]));
    }
      }
      if (e == 1) {
        avo = WS_AT + (u32)(32 * cb + (tid >> 4)) * AS + 512 * rb + 16 * (tid & 15);
        asm volatile("" : "+v"(avo));
      }
      if (e == HPUB) {
        STAMP(21);
        publish<0>(rws, WS_FH + 128 * mid + 16 * wave, tagv);
        STAMP(22);
      }
      SB0;
    }
    PRIO_OFF(2);
    if (cj == 2) {
      const s16x8 bm = *(LAS s16x8*)(lds + bb[0] - 512);
#pragma unroll
      for (int jt = 0; jt < 8; ++jt) MF16(ya[jt], RDA(0, jt + 1), bm);
    }
    SB0;
#pragma unroll
    for (int jt = 0; jt < 8; ++jt) {
#pragma unroll
      for (int e = 0; e <= jt; ++e) MF16(ya[jt], a1[e], b1[jt - e]);
      SB0;
      if (jt < 5) { a1[jt + 3] = RDA(1, jt + 3); b1[jt + 3] = RDB(1, jt + 3); }
      if (jt == FHC) {
        STAMP(9);
        asm volatile("" : "+v"(fh) :: "memory");
        hok = all_eq(fh, tag);
      }
      if (jt >= FHC) {
#pragma unroll
        for (int i = G2_PREC * (jt - FHC) / (8 - FHC); i < G2_PREC * (jt + 1 - FHC) / (8 - FHC); ++i) { LDH(i); SB0; }
      }
      if (jt > 0) {
        *(LAS f32x4*)(lds + exw) = ya[jt - 1];
        exw += exs;
      }
      SB0;
    }
    *(LAS f32x4*)(lds + exw) = ya[7];
    STAMP(7);
    if (__builtin_expect(!hok, 0)) {
      FALLBACK(2);
      wait_flags(P.ws + fho, tag);
#pragma unroll
      for (int i = 0; i < G2_PREC; ++i) { LDH(i); SB0; }
    }
#pragma unroll
    for (int i = G2_PREC; i < G2_PRE0; ++i) { LDH(i); SB0; }
    gv = __builtin_amdgcn_raw_buffer_load_b32(rws, 4 * lane, WS_G + 256 * rb, 16);
    STAMP(26);
    BARL;
    f32x2 ev[2][2][4];
    u32 uu[2][2];
    asm volatile("ds_read_b64 %0, %20\n\tds_read_b64 %1, %20 offset:1024\n\tds_read_b64 %2, %20 offset:2048\n\tds_read_b64 %3, %21\n\t"
                 "ds_read_b64 %4, %20 offset:3072\n\tds_read_b64 %5, %20 offset:4096\n\tds_read_b64 %6, %20 offset:5120\n\t"
                 "ds_read_b64 %7, %21 offset:1024\n\tds_read_b32 %16, %22\n\tds_read_b32 %17, %22 offset:4672\n\t"
                 "ds_read_b64 %8, %20 offset:512\n\tds_read_b64 %9, %20 offset:1536\n\tds_read_b64 %10, %20 offset:2560\n\t"
                 "ds_read_b64 %11, %21 offset:512\n\tds_read_b64 %12, %20 offset:3584\n\tds_read_b64 %13, %20 offset:4608\n\t"
                 "ds_read_b64 %14, %20 offset:5632\n\tds_read_b64 %15, %21 offset:1536\n\tds_read_b32 %18, %22 offset:256\n\t"
                 "ds_read_b32 %19, %22 offset:4928\n\ts_waitcnt lgkmcnt(0)"
                 : "=&v"(ev[0][0][0]), "=&v"(ev[0][0][1]), "=&v"(ev[0][0][2]), "=&v"(ev[0][0][3]), "=&v"(ev[0][1][0]),
                   "=&v"(ev[0][1][1]), "=&v"(ev[0][1][2]), "=&v"(ev[0][1][3]), "=&v"(ev[1][0][0]), "=&v"(ev[1][0][1]),
                   "=&v"(ev[1][0][2]), "=&v"(ev[1][0][3]), "=&v"(ev[1][1][0]), "=&v"(ev[1][1][1]), "=&v"(ev[1][1][2]),
                   "=&v"(ev[1][1][3]), "=&v"(uu[0][0]), "=&v"(uu[0][1]), "=&v"(uu[1][0]), "=&v"(uu[1][1])
                 : "v"(exr), "v"(exo), "v"(uur) : "memory");
    static_assert(UP_STRIDE == 4672, "");
#pragma unroll
    for (int h = 0; h < 2; ++h) {
      f32x2 y[2];
#pragma unroll
      for (int c = 0; c < 2; ++c)
        y[c] = ((ev[h][c][0] + ev[h][c][1]) + (ev[h][c][2] + ev[h][c][3])) +
               f32x2{bf_lo(uu[h][c]), bf_hi(uu[h][c])} * (c ? bf_hi(p_dsk) : bf_lo(p_dsk));
      __builtin_amdgcn_raw_buffer_store_b128(u32x4{pk_bf16(y[0].x, y[1].x), (u32)tag, pk_bf16(y[0].y, y[1].y), (u32)tag}, rws,
                                             ato + 1024 * h, 0, 16);
    }
    SB0;
    STAMP(30);
  }
  STAMP(8);
  {
    u32x4 avl[2];
#define LDA() do { avl[0] = __builtin_amdgcn_raw_buffer_load_b128(rws, avo, 0, 16); \
                   avl[1] = __builtin_amdgcn_raw_buffer_load_b128(rws, avo + 256, 0, 16); } while (0)
#define AOK() all_true(avl[0][1] == (u32)tag && avl[0][3] == (u32)tag && avl[1][1] == (u32)tag && avl[1][3] == (u32)tag)
    constexpr u32 pbase[4] = {49152, 81920, 0, 114688};
    bool aok = false;
    SB0;
    f32x4 oa[2][4];
#define PUTP(tt, i) *(LAS f32x4*)(lds + ppa[i] + ((tt) == 1 ? 32768 : 0)) = oa[(tt) & 1][i]
#pragma unroll
    for (int tt = 0; tt < 4; ++tt) {
#pragma unroll
      for (int k = 0; k < 7; ++k) {
        const int i = 7 * tt + k;
#pragma unroll
        for (int dt4 = 0; dt4 < 4; ++dt4) {
          if (k == 0) oa[tt & 1][dt4] = f32x4{0.f, 0.f, 0.f, 0.f};
          MF16(oa[tt & 1][dt4], wdf[k][dt4], hf[i]);
        }
        SB0;
        if (i + G2_LA >= G2_PRE0 && i + G2_LA < 28) LDH(i + G2_LA);
        if (tt > 0 && k < 4) {
          if (tt == 3) ppa[k] -= 49152;
          PUTP(tt - 1, k);
        }
        if (tt == 2 && k == 5) BARL;
        if (i == FAP) LDA();
        if (i == FAC) {
          aok = AOK();
          if (__builtin_expect(!aok, 0)) LDA();
        }
        if (tt == 3 && k >= 3) { ppa[k - 3] += 114688; asm volatile("" : "+v"(ppa[k - 3])); }
        SB0;
      }
      if (tt == 0) STAMP(23);
      if (tt == 1) STAMP(24);
      if (tt == 2) STAMP(25);
    }
    PUTP(3, 0); PUTP(3, 1); PUTP(3, 2); PUTP(3, 3);
    STAMP(10);
    if (__builtin_expect(!aok, 0)) {
      FALLBACK(3);
#pragma unroll 1
      for (int it = 0; it < SPIN_MAX && !AOK(); ++it) LDA();
    }
    *(LAS u32x2*)(lds + LD_AT + 272 * (tid >> 4) + 8 * (tid & 15)) = u32x2{avl[0][0], avl[0][2]};
    *(LAS u32x2*)(lds + LD_AT + 272 * (tid >> 4) + 8 * (tid & 15) + 128) = u32x2{avl[1][0], avl[1][2]};
    if (wave == 0) *(LAS u32*)(lds + LD_AT + 9216 + 4 * lane) = gv;
    const int dh = wave & 1;
    const int tw = ((wave >> 1) + ROT * cb) & 3, t = 64 * rb + 16 * tw + r16;
    f32x4 b0, b1;
    const u32 ro = (wave < 2 ? pbase[0] : wave < 4 ? pbase[1] : wave < 6 ? pbase[2] : pbase[3]) + 256 * r16;
    u32 r0 = ro + 16 * ((8 * dh + 2 * q) ^ r16), r1 = ro + 16 * ((8 * dh + 2 * q + 1) ^ r16);
    asm volatile("" : "+v"(r0), "+v"(r1));
    auto reduce = [&]() {
      f32x4 p[16];
#pragma unroll
      for (int w = 0; w < 8; ++w) { p[2 * w] = *(LAS f32x4*)(lds + 4096 * w + r0); p[2 * w + 1] = *(LAS f32x4*)(lds + 4096 * w + r1); }
      b0 = ((p[0] + p[2]) + (p[4] + p[6])) + ((p[8] + p[10]) + (p[12] + p[14]));
      b1 = ((p[1] + p[3]) + (p[5] + p[7])) + ((p[9] + p[11]) + (p[13] + p[15]));
    };
    if (wave < 4) {
      reduce();
      BARL;
    } else {
      BARL;
      reduce();
    }
    STAMP(12);
    u32 av[4];
#pragma unroll
    for (int i = 0; i < 4; ++i) av[i] = *(LAS u32*)(lds + LD_AT + 272 * (16 * dh + 4 * q + i) + 4 * (16 * tw + r16));
    const float g = *(LAS float*)(lds + LD_AT + 9216 + 4 * (16 * tw + r16));
    const float og = 1.f - g;
    const f32x4 m0 = f32x4{bf_lo(av[0]), bf_hi(av[0]), bf_lo(av[1]), bf_hi(av[1])} * g + b0 * og;
    const f32x4 m1 = f32x4{bf_lo(av[2]), bf_hi(av[2]), bf_lo(av[3]), bf_hi(av[3])} * g + b1 * og;
    const u32x4 o = {pk_bf16(m0.xy), pk_bf16(m0.zw), pk_bf16(m1.xy), pk_bf16(m1.zw)};
    *(u32x4*)(P.out + (size_t)t * D_ + 64 * cb + 32 * dh + 8 * q) = o;
    STAMP(13);
    STAMP_OUT;
  }
}

extern "C" void kernel_launch(void* const* d_in, const int* in_sizes, int n_in, void* d_out, int out_size, void* d_ws,
                              size_t ws_size, hipStream_t stream) {
  Params P;
  P.u = (const u16*)d_in[0];
  P.are = (const u16*)d_in[1];
  P.aim = (const u16*)d_in[2];
  P.bre = (const u16*)d_in[3];
  P.bim = (const u16*)d_in[4];
  P.cre = (const u16*)d_in[5];
  P.cim = (const u16*)d_in[6];
  P.ldt = (const u16*)d_in[7];
  P.dsk = (const u16*)d_in[8];
  P.wg = (const u16*)d_in[10];
  P.wu = (const u16*)d_in[11];
  P.wd = (const u16*)d_in[12];
  P.gw = (const u16*)d_in[13];
  P.out = (u16*)d_out;
  P.ws = (char*)d_ws;
  gated_mix_kernel<<<256, 512, 0, stream>>>(P);
}
```

```cpp
#include <hip/hip_runtime.h>

typedef unsigned short u16;
typedef unsigned int u32;
typedef unsigned long long u64;
typedef __attribute__((ext_vector_type(8))) short s16x8;
typedef __attribute__((ext_vector_type(4))) short s16x4;
typedef __attribute__((ext_vector_type(2))) float f32x2;
typedef __attribute__((ext_vector_type(4))) float f32x4;
typedef __attribute__((ext_vector_type(16))) float f32x16;
typedef __attribute__((ext_vector_type(2))) u32 u32x2;
typedef __attribute__((ext_vector_type(4))) u32 u32x4;
typedef __attribute__((ext_vector_type(2))) __bf16 bf16x2;
#define LAS __attribute__((address_space(3)))
#define GAS __attribute__((address_space(1)))
#define DI __device__ __forceinline__
#define SB0 __builtin_amdgcn_sched_barrier(0)
#define SBV __builtin_amdgcn_sched_barrier(0x8 | 0x4 | 0x10 | 0x80)

namespace {
constexpr int L_ = 2048, D_ = 448, N_ = 256, F_ = 1792;
constexpr u32 WS_EPOCH = 0;
constexpr u32 WS_FW = 1024;
constexpr u32 WS_FG = WS_FW + 32 * 128;
constexpr u32 WS_FH = WS_FG + 32 * 128;
constexpr u32 WS_FA = WS_FH + 224 * 128;
constexpr u32 WS_G = 98304;
constexpr u32 WS_UT = 131072;
constexpr u32 TS = L_ * 2 + 128;
constexpr u32 WS_AT = WS_UT + D_ * TS;
constexpr u32 WS_WD = WS_AT + D_ * TS;
constexpr u32 WS_H = WS_WD + F_ * D_ * 2;
constexpr u32 WS_END = WS_H + L_ * F_ * 2;
constexpr u32 WS_PROF = 16u << 20;
constexpr u32 LD_TB = 98304, TB_STRIDE = 5184;
constexpr u32 LD_KR = 155648, KR_STRIDE = 3584;
constexpr u32 LD_RL = 147456;
constexpr u32 LD_RK = 147456;
constexpr u32 LD_CP = 49152, CP_STRIDE = 35328;
constexpr u32 LD_UP = 119808, UP_STRIDE = 4672;
constexpr u32 LD_EX = 0;
constexpr u32 LD_AT = 147456;
constexpr u32 SY_HCNT = 163584, SY_ACNT = SY_HCNT + 4, SY_XCNT = SY_HCNT + 8;
constexpr int SPIN_MAX = 1 << 16, SPIN_MAX_LDS = 1 << 20;
#define CODE_PREFETCH_LINES 0
struct Params {
  const u16 *u, *are, *aim, *bre, *bim, *cre, *cim, *ldt, *dsk, *wg, *wu, *wd, *gw;
  u16* out;
  char* ws;
};

DI float bf_lo(u32 x) { return __uint_as_float(x << 16); }
DI float bf_hi(u32 x) { return __uint_as_float(x & 0xffff0000u); }
DI u32 pk_bf16(float a, float b) {
  f32x2 v = {a, b};
  return __builtin_bit_cast(u32, __builtin_convertvector(v, bf16x2));
}
DI u32 pk_bf16(f32x2 v) { return __builtin_bit_cast(u32, __builtin_convertvector(v, bf16x2)); }
struct cf { float x, y; };
DI cf cmul(cf a, cf b) { return {__builtin_fmaf(a.x, b.x, -(a.y * b.y)), __builtin_fmaf(a.x, b.y, a.y * b.x)}; }
DI cf cexp2pi(float x, float y) {
  const float e = __builtin_amdgcn_exp2f(x), f = __builtin_amdgcn_fractf(y);
  return {e * __builtin_amdgcn_cosf(f), e * __builtin_amdgcn_sinf(f)};
}
typedef __attribute__((ext_vector_type(2))) _Float16 h2;
typedef __attribute__((ext_vector_type(8))) _Float16 h8;
DI h2 pkh(cf a) { f32x2 v = {a.x, a.y}; return __builtin_convertvector(v, h2); }
template <int N> DI void cmuln(cf (&r)[N], const cf (&a)[N], const cf (&b)[N]) {
  float t[N], u[N];
#pragma unroll
  for (int i = 0; i < N; ++i) { t[i] = a[i].y * b[i].y; u[i] = a[i].y * b[i].x; }
  SBV;
#pragma unroll
  for (int i = 0; i < N; ++i) r[i] = {__builtin_fmaf(a[i].x, b[i].x, -t[i]), __builtin_fmaf(a[i].x, b[i].y, u[i])};
  SBV;
}
#define PKM " op_sel_hi:[0,1]\n\t"
#define PKF " op_sel:[1,1,0] op_sel_hi:[1,0,1] neg_lo:[0,1,0]\n\t"
DI u32x4 cmulh4(u32x4 a, u32x4 b) {
  u32 r0, r1, r2, r3;
  const u32 a0 = a[0], a1 = a[1], a2 = a[2], a3 = a[3], b0 = b[0], b1 = b[1], b2 = b[2], b3 = b[3];
  asm("v_pk_mul_f16 %0, %4, %8" PKM "v_pk_mul_f16 %1, %5, %9" PKM "v_pk_mul_f16 %2, %6, %10" PKM "v_pk_mul_f16 %3, %7, %11" PKM
      "v_pk_fma_f16 %0, %4, %8, %0" PKF "v_pk_fma_f16 %1, %5, %9, %1" PKF "v_pk_fma_f16 %2, %6, %10, %2" PKF
      "v_pk_fma_f16 %3, %7, %11, %3 op_sel:[1,1,0] op_sel_hi:[1,0,1] neg_lo:[0,1,0]"
      : "=&v"(r0), "=&v"(r1), "=&v"(r2), "=&v"(r3)
      : "v"(a0), "v"(a1), "v"(a2), "v"(a3), "v"(b0), "v"(b1), "v"(b2), "v"(b3));
  return u32x4{r0, r1, r2, r3};
}
#define H8(v) __builtin_bit_cast(h8, v)
template <int CTRL, int RM> DI float dpp_max(float x) {
  return __builtin_fmaxf(x, __int_as_float(__builtin_amdgcn_update_dpp(0, __float_as_int(x), CTRL, RM, 0xf, true)));
}
template <int CTRL, int RM> DI float dpp_add(float x) {
  return x + __int_as_float(__builtin_amdgcn_update_dpp(0, __float_as_int(x), CTRL, RM, 0xf, true));
}
DI float wave_sum63(float x) {
  x = dpp_add<0x111, 0xf>(x); x = dpp_add<0x112, 0xf>(x); x = dpp_add<0x114, 0xf>(x); x = dpp_add<0x118, 0xf>(x);
  x = dpp_add<0x142, 0xa>(x); x = dpp_add<0x143, 0xc>(x);
  return x;
}
DI float wave_max(float x) {
  x = dpp_max<0x111, 0xf>(x); x = dpp_max<0x112, 0xf>(x); x = dpp_max<0x114, 0xf>(x); x = dpp_max<0x118, 0xf>(x);
  x = dpp_max<0x142, 0xa>(x); x = dpp_max<0x143, 0xc>(x);
  return __int_as_float(__builtin_amdgcn_readlane(__float_as_int(x), 63));
}
DI void pk4_mul_s(f32x2 (&r)[4], const f32x2 (&a)[4], f32x2 c) {
  asm("v_pk_mul_f32 %0, %4, %8\n\tv_pk_mul_f32 %1, %5, %8\n\tv_pk_mul_f32 %2, %6, %8\n\tv_pk_mul_f32 %3, %7, %8"
      : "=&v"(r[0]), "=&v"(r[1]), "=&v"(r[2]), "=&v"(r[3]) : "v"(a[0]), "v"(a[1]), "v"(a[2]), "v"(a[3]), "s"(c));
}
DI void pk4_mul(f32x2 (&r)[4], const f32x2 (&a)[4], const f32x2 (&b)[4]) {
  asm("v_pk_mul_f32 %0, %4, %8\n\tv_pk_mul_f32 %1, %5, %9\n\tv_pk_mul_f32 %2, %6, %10\n\tv_pk_mul_f32 %3, %7, %11"
      : "=&v"(r[0]), "=&v"(r[1]), "=&v"(r[2]), "=&v"(r[3])
      : "v"(a[0]), "v"(a[1]), "v"(a[2]), "v"(a[3]), "v"(b[0]), "v"(b[1]), "v"(b[2]), "v"(b[3]));
}
DI void pk4_add1(f32x2 (&r)[4]) {
  asm("s_nop 0\n\tv_pk_add_f32 %0, %0, 1.0 op_sel_hi:[1,0]\n\tv_pk_add_f32 %1, %1, 1.0 op_sel_hi:[1,0]\n\t"
      "v_pk_add_f32 %2, %2, 1.0 op_sel_hi:[1,0]\n\tv_pk_add_f32 %3, %3, 1.0 op_sel_hi:[1,0]"
      : "+v"(r[0]), "+v"(r[1]), "+v"(r[2]), "+v"(r[3]));
}
DI f32x4 add4(f32x4 a, f32x4 b) {
  float r0, r1, r2, r3;
  const float a0 = a[0], a1 = a[1], a2 = a[2], a3 = a[3], b0 = b[0], b1 = b[1], b2 = b[2], b3 = b[3];
  asm("v_add_f32 %0, %4, %8\n\tv_add_f32 %1, %5, %9\n\tv_add_f32 %2, %6, %10\n\tv_add_f32 %3, %7, %11"
      : "=&v"(r0), "=&v"(r1), "=&v"(r2), "=&v"(r3) : "v"(a0), "v"(a1), "v"(a2), "v"(a3), "v"(b0), "v"(b1), "v"(b2), "v"(b3));
  return f32x4{r0, r1, r2, r3};
}
DI bool all_eq(u64 a, u64 b) { return __all(a == b); }
DI float dexpf(float x) { return expf(x); }
DI float rbf(float x) { return bf_lo(pk_bf16(x, 0.f)); }
DI __amdgpu_buffer_rsrc_t mkrs(const void* p, u32 bytes) {
  return __builtin_amdgcn_make_buffer_rsrc((void*)p, 0, bytes, 0x00020000);
}
DI void dma16(u32 ldsaddr, u32 voff, __amdgpu_buffer_rsrc_t rs, u32 soff) {
  asm volatile("s_mov_b32 m0, %0\n\ts_nop 0\n\tbuffer_load_dwordx4 %1, %2, %3 offen lds"
               :: "s"(ldsaddr), "v"(voff), "s"(rs), "s"(soff) : "memory");
}
DI void gdma4(u32 ldsaddr, u32 voff, const void* base) {
  asm volatile("s_mov_b32 m0, %0\n\ts_nop 0\n\tglobal_load_lds_dword %1, %2" :: "s"(ldsaddr), "v"(voff), "s"(base) : "memory");
}
DI void gdma16(u32 ldsaddr, u32 voff, const void* base) {
  asm volatile("s_mov_b32 m0, %0\n\ts_nop 0\n\tglobal_load_lds_dwordx4 %1, %2" :: "s"(ldsaddr), "v"(voff), "s"(base) : "memory");
}
#define CAS __attribute__((address_space(4)))
DI u64 ld_flag(const char* p) { return __hip_atomic_load((const GAS u64*)p, __ATOMIC_RELAXED, __HIP_MEMORY_SCOPE_AGENT); }
DI void st_flag(char* p, u64 v) { __hip_atomic_store((GAS u64*)p, v, __ATOMIC_RELAXED, __HIP_MEMORY_SCOPE_AGENT); }
DI void wait_flags(const char* p, u64 tag) {
#pragma unroll 1
  for (int it = 0; it < SPIN_MAX; ++it) {
    if (__all(ld_flag(p) == tag)) break;
    __builtin_amdgcn_s_sleep(2);
  }
  asm volatile("" ::: "memory");
}
DI void lds_wait_ge(LAS char* lds, u32 off, u32 v) {
#pragma unroll 1
  for (int it = 0; it < SPIN_MAX_LDS; ++it) {
    u32 x = __builtin_amdgcn_readfirstlane(*(volatile LAS u32*)(lds + off));
    if (x >= v) break;
    __builtin_amdgcn_s_sleep(1);
  }
  asm volatile("" ::: "memory");
}
DI u32 lds_add(LAS char* lds, u32 off, int lane) {
  u32 old = 0;
  if (lane == 0) old = __hip_atomic_fetch_add((LAS u32*)(lds + off), 1u, __ATOMIC_RELAXED, __HIP_MEMORY_SCOPE_WORKGROUP);
  return __builtin_amdgcn_readfirstlane(old);
}
DI s16x8 tr_frag(LAS char* lds, u32 a0, u32 a1) {
  s16x4 x = __builtin_amdgcn_ds_read_tr16_b64_v4i16((LAS s16x4*)(lds + a0));
  s16x4 y = __builtin_amdgcn_ds_read_tr16_b64_v4i16((LAS s16x4*)(lds + a1));
  return __builtin_shufflevector(x, y, 0, 1, 2, 3, 4, 5, 6, 7);
}

#define STAMP(i)
#define STAMP_DECL
#define PROF_ARGS
#define PROF_PASS
#define STAMP_OUT

DI void helper_block(const Params& P, LAS char* lds, int s, int tid, int lane, int wave) {
  STAMP_DECL;
  STAMP(0);
  constexpr u32 RS = 928;
  const int r = tid >> 3, c8 = tid & 7;
  const bool dow = s < 28;
  u32x4 xw[7], xu[7], gw[7];
  const u32x4* wp = (const u32x4*)(P.wd + (size_t)(64 * (dow ? s : 0) + r) * D_) + c8;
#pragma unroll
  for (int i = 0; i < 7; ++i) xw[i] = wp[8 * i];
  SB0;
  SB0;
  const u32x4* up = (const u32x4*)(P.u + (size_t)(64 * s + r) * D_) + c8;
#pragma unroll
  for (int i = 0; i < 7; ++i) xu[i] = up[8 * i];
#pragma unroll
  for (int i = 0; i < 7; ++i) gw[i] = ((const u32x4*)P.gw)[c8 + 8 * i];
  const u64 tag = ld_flag(P.ws + WS_EPOCH) + 1;
  SB0;
  SB0;
#pragma unroll
  for (int i = 0; i < 7; ++i) *(LAS u32x4*)(lds + 65536 + r * RS + 16 * (c8 + 8 * i)) = xw[i];
  asm volatile("s_waitcnt lgkmcnt(0)\n\ts_barrier" ::: "memory");
  STAMP(2);
  auto rws = mkrs(P.ws, WS_END);
  const int q = lane >> 4, a = (lane & 15) >> 2, p = lane & 3;
  u32 wbase = dow ? WS_WD + 8192 * s + lane * 16 : WS_END;
  asm volatile("" : "+v"(wbase));
#pragma unroll
  for (int i = 0; i < 7; ++i) {
    const int un = wave * 7 + i, dt = un >> 1, kh = un & 1;
    const u32 ad = 65536 + (32 * kh + 16 * (q >> 1) + 4 * (q & 1) + a) * RS + 32 * dt + 8 * p;
    __builtin_amdgcn_raw_buffer_store_b128(__builtin_bit_cast(u32x4, tr_frag(lds, ad, ad + 8 * RS)), rws,
        wbase + (((dt >> 2) * 56 + kh) * 4 + (dt & 3)) * 1024, 0, 16);
  }
  SB0;
  STAMP(3);
#pragma unroll
  for (int i = 0; i < 7; ++i) *(LAS u32x4*)(lds + r * RS + 16 * (c8 + 8 * i)) = xu[i];
  asm volatile("s_waitcnt vmcnt(0) lgkmcnt(0)\n\ts_barrier" ::: "memory");
  if (tid == 0) st_flag(P.ws + WS_FW + 128 * s, tag);
  STAMP(4);
#pragma unroll
  for (int i = 0; i < 7; ++i) {
    const int d8 = 8 * (wave * 7 + i);
    const u32 ad = (8 * (2 * q + (p >> 1)) + a) * RS + 2 * d8 + 8 * (p & 1);
    u32x4 v = __builtin_bit_cast(u32x4, tr_frag(lds, ad, ad + 4 * RS));
    LAS char* st = lds + 65536 + 1152 * (wave * 7 + i);
    *(LAS u32x4*)(st + 144 * (lane & 7) + 16 * (2 * q + ((lane >> 3) & 1))) = v;
    v = *(LAS u32x4*)(st + 144 * (lane >> 3) + 16 * (lane & 7));
    __builtin_amdgcn_raw_buffer_store_b128(v, rws, WS_UT + (d8 + (lane >> 3)) * TS + 128 * s + 16 * (lane & 7), 0, 16);
  }
  float gs = 0.f;
#pragma unroll
  for (int i = 0; i < 7; ++i)
#pragma unroll
    for (int j = 0; j < 4; ++j) {
      const u32 pr = pk_bf16(bf_lo(xu[i][j]) * bf_lo(gw[i][j]), bf_hi(xu[i][j]) * bf_hi(gw[i][j]));
      gs += bf_lo(pr) + bf_hi(pr);
    }
  gs += __shfl_xor(gs, 1);
  gs += __shfl_xor(gs, 2);
  gs += __shfl_xor(gs, 4);
  if (c8 == 0) {
    const float g = rbf(__builtin_amdgcn_rcpf(1.f + __expf(-rbf(gs))));
    __builtin_amdgcn_raw_buffer_store_b32(__float_as_uint(g), rws, WS_G + 4 * (64 * s + r), 0, 16);
  }
  asm volatile("s_waitcnt vmcnt(0)" ::: "memory");
  __syncthreads();
  if (tid == 0) st_flag(P.ws + WS_FG + 128 * s, tag);
  STAMP(1);
  STAMP_OUT;
  if (s == 0 && wave == 0) {
#pragma unroll 1
    for (int i = 0; i < 4; ++i) wait_flags(P.ws + WS_FG + 128 * (lane + 64 * i), tag);
    if (lane == 0) st_flag(P.ws + WS_EPOCH, tag);
  }
}

#define MF32(acc, a, b) acc = __builtin_amdgcn_mfma_f32_32x32x16_bf16(a, b, acc, 0, 0, 0)
#define MF16(acc, a, b) acc = __builtin_amdgcn_mfma_f32_16x16x32_bf16(a, b, acc, 0, 0, 0)
#define MFH(acc, a, b) acc = __builtin_amdgcn_mfma_f32_16x16x32_f16(a, b, acc, 0, 0, 0)
#define BARL do { SB0; asm volatile("s_waitcnt lgkmcnt(0)\n\ts_barrier" ::: "memory"); SB0; } while (0)
template <int N> DI void wait_vm() { asm volatile("s_waitcnt vmcnt(%0)" :: "n"(N) : "memory"); }
template <int N> struct IC { static constexpr int v = N; };

struct G1 {
  __amdgpu_buffer_rsrc_t rx, rg, ru;
  u32 vx[2], vw[2], wofs;
  u32 xa[3][4], wa[3][2];
};
struct G1Frag { s16x8 a[2], b[2]; };
template <int C, int I>
DI void g1_dma(const G1& d) {
  if constexpr (C < 7) {
    const u32 dst = (C % 3) * 49152u + (I >> 1) * 16384u + (I & 1) * 1024u + d.wofs;
    if constexpr (I < 2) dma16(dst, d.vx[I], d.rx, C * 128u);
    else dma16(dst, d.vw[I & 1], I < 4 ? d.rg : d.ru, C * 64u * 3584u);
  }
}
template <int C, int KS, int I>
DI void g1_rd(G1Frag& f, LAS char* lds, const G1& d) {
  if constexpr (C < 7) {
    if constexpr (I == 0) {
      f.b[0] = *(LAS s16x8*)(lds + d.xa[C % 3][KS]);
      f.b[1] = *(LAS s16x8*)(lds + d.xa[C % 3][KS] + 4096u);
    } else {
      f.a[I - 1] = tr_frag(lds, d.wa[C % 3][0] + I * 16384u + KS * 4096u, d.wa[C % 3][1] + I * 16384u + KS * 4096u);
    }
  }
}
template <int S, class X>
DI void g1_step(f32x16 (&acc)[2][2], G1Frag (&f)[3], LAS char* lds, G1& d, X&& extra) {
  constexpr int NC = (S + 2) >> 2, NK = (S + 2) & 3;
  constexpr int X0 = S < 2 ? 12 + 3 * S : ((S - 2) & 3) < 3 ? 6 * (((S - 2) >> 2) + 3) + 2 * ((S - 2) & 3) : 99;
  constexpr int X2 = S < 2 ? X0 + 2 : 99;
  G1Frag& cur = f[S % 3];
  G1Frag& nxt = f[(S + 2) % 3];
  extra(IC<4 * S>{});
  SB0;
  MF32(acc[0][0], cur.a[0], cur.b[0]); SB0;
  g1_rd<NC, NK, 0>(nxt, lds, d);
  g1_dma<X0 / 6, X0 % 6>(d);
  extra(IC<4 * S + 1>{}); SB0;
  MF32(acc[0][1], cur.a[0], cur.b[1]); SB0;
  g1_rd<NC, NK, 1>(nxt, lds, d);
  g1_dma<X2 / 6, X2 % 6>(d);
  extra(IC<4 * S + 2>{}); SB0;
  MF32(acc[1][0], cur.a[1], cur.b[0]); SB0;
  g1_rd<NC, NK, 2>(nxt, lds, d);
  g1_dma<(X0 + 1) / 6, (X0 + 1) % 6>(d);
  extra(IC<4 * S + 3>{}); SB0;
  MF32(acc[1][1], cur.a[1], cur.b[1]); SB0;
  extra(IC<1000 + S>{}); SB0;
}
template <int C, class X>
DI void g1_chunk(f32x16 (&acc)[2][2], G1Frag (&f)[3], LAS char* lds, G1& d, X&& extra) {
  g1_step<4 * C>(acc, f, lds, d, extra);
  g1_step<4 * C + 1>(acc, f, lds, d, extra);
  g1_step<4 * C + 2>(acc, f, lds, d, extra);
  g1_step<4 * C + 3>(acc, f, lds, d, extra);
}

}

#define ROT 1
#define NWARM 1
#define LW_STEP 2
#define LW_EARLY 20
#define HPUB 3
#define LDWD(k) wdf[(k) >> 2][(k) & 3] = __builtin_bit_cast(s16x8, __builtin_amdgcn_raw_buffer_load_b128( \
    rws, lane * 16, WS_WD + ((cb * 56 + 7 * wave) * 4 + (k)) * 1024, 16))
#define FHP 6
#define ABL 0
#define G2_PRE0 4
#define APUB 4
#define G2_PRE 10

__global__ __launch_bounds__(512, 2) void gated_mix_kernel(Params P) {
  __shared__ __attribute__((aligned(16))) char smem[163840];
  LAS char* lds = (LAS char*)smem;
  const int tid = threadIdx.x, lane = tid & 63;
  const int wave = __builtin_amdgcn_readfirstlane(tid >> 6);
  const int bid = blockIdx.x;
  STAMP_DECL;
  STAMP(0);
  if (bid < 32) {
    helper_block(P, lds, bid, tid, lane, wave);
    return;
  }
  const int xcd = bid & 7, slot = (bid >> 3) - 4;
  const int tb = 4 * (xcd >> 1) + slot / 7, fb = 7 * (xcd & 1) + slot % 7;
  const int mid = tb * 14 + fb;
  const int rb = 4 * xcd + slot / 7, cb = slot % 7;
  const int ch = wave >> 2, d0 = 2 * mid;
  const int q = lane >> 4, r16 = lane & 15;

  const u32 tbl = LD_TB + TB_STRIDE * wave;
  {
    u64 po = mid * 1024 + wave * 128 + (lane & 7) * 16;
    u32 m0v = tbl + 4096;
    asm volatile("" : "+v"(po), "+s"(m0v));
    SB0;
    u32 lo, hi;
#define LO(p) "s"((u32)(u64)(p))
#define HI(p) "s"((u32)((u64)(p) >> 32))
    asm volatile("v_mov_b32 %0, %2\n\tv_mov_b32 %1, %3\n\ts_lshl_b64 exec, exec, 8\n\t"
                 "v_mov_b32 %0, %4\n\tv_mov_b32 %1, %5\n\ts_lshl_b64 exec, exec, 8\n\t"
                 "v_mov_b32 %0, %6\n\tv_mov_b32 %1, %7\n\ts_lshl_b64 exec, exec, 8\n\t"
                 "v_mov_b32 %0, %8\n\tv_mov_b32 %1, %9\n\ts_lshl_b64 exec, exec, 8\n\t"
                 "v_mov_b32 %0, %10\n\tv_mov_b32 %1, %11\n\ts_lshl_b64 exec, exec, 8\n\t"
                 "v_mov_b32 %0, %12\n\tv_mov_b32 %1, %13\n\ts_mov_b64 exec, -1"
                 : "=&v"(lo), "=&v"(hi)
                 : LO(P.are), HI(P.are), LO(P.aim), HI(P.aim), LO(P.bre), HI(P.bre), LO(P.bim), HI(P.bim), LO(P.cre), HI(P.cre),
                   LO(P.cim), HI(P.cim) : "scc");
    asm volatile("s_mov_b32 m0, %0\n\ts_nop 0\n\tglobal_load_lds_dwordx4 %1, off"
                 :: "s"(m0v), "v"((((u64)hi << 32) | lo) + po) : "memory");
  }
  u64 tag = *(const CAS u64*)(P.ws + WS_EPOCH);
  const u32 p_ldt = *(const CAS u32*)(P.ldt + d0), p_dsk = *(const CAS u32*)(P.dsk + d0);
  SB0;
  G1 d;
  d.rx = mkrs(P.u + (size_t)tb * 128 * D_, 128 * D_ * 2);
  d.rg = mkrs(P.wg + fb * 128, (D_ * F_ - fb * 128) * 2);
  d.ru = mkrs(P.wu + fb * 128, (D_ * F_ - fb * 128) * 2);
  d.wofs = 2048u * wave;
#pragma unroll
  for (int i = 0; i < 2; ++i) {
    d.vx[i] = (16 * wave + 8 * i + (lane >> 3)) * 896 + 16 * ((lane & 7) ^ ((4 * i + q) & 7));
    d.vw[i] = (8 * wave + 4 * i + q) * 3584 + 16 * (r16 ^ ((q << 2) | ((2 * wave + i) & 3)));
  }
  g1_dma<0, 0>(d); g1_dma<0, 2>(d); g1_dma<0, 4>(d); g1_dma<0, 1>(d); g1_dma<0, 3>(d); g1_dma<0, 5>(d);
  auto rws = mkrs(P.ws, WS_END);
  STAMP(14);
  const int wf = wave & 3, wt = wave >> 2, hh = lane >> 5;
#pragma unroll
  for (int st = 0; st < 1; ++st) {
#pragma unroll
    for (int ks = 0; ks < 4; ++ks) {
      d.xa[st][ks] = 49152 * st + 128 * (64 * wt + (lane & 31)) + 16 * ((2 * ks + hh) ^ ((lane >> 1) & 7));
      asm volatile("" : "+v"(d.xa[st][ks]));
    }
    const int blk = q & 1, a = r16 >> 2, p = lane & 3;
#pragma unroll
    for (int t = 0; t < 2; ++t) {
      d.wa[st][t] = 49152 * st + 256 * (8 * hh + 4 * t + a) + 16 * ((4 * wf + 2 * blk + (p >> 1)) ^ ((a << 2) | ((2 * hh + t) & 3))) + 8 * (p & 1);
      asm volatile("" : "+v"(d.wa[st][t]));
    }
  }
  const int tl = tid & 255;
  u32 kofs[8], ra[7];
  {
    const int hi = r16 >> 2, lo = r16 & 3;
    const int row[7] = {4 + hi, lo, hi ? 10 + hi : 0, lo ? 7 + lo : 0, hi == 1 ? 11 : 0, r16 < 8 ? 15 : r16 < 12 ? 14 : 16, 17};
#pragma unroll
    for (int i = 0; i < 7; ++i) {
      ra[i] = tbl + 288 * row[i] + 16 * q;
      asm volatile("" : "+v"(ra[i]));
    }
  }
  const float dt = rbf(dexpf(ch ? bf_hi(p_ldt) : bf_lo(p_ldt)));
  *(LAS h2*)(lds + tbl + 4 * lane) = h2{(_Float16)1.f, (_Float16)0.f};
  STAMP(15);
  SB0;
  wait_vm<6>();
  u32 prm[6];
#pragma unroll
  for (int i = 0; i < 6; ++i) prm[i] = *(LAS u16*)(lds + tbl + 4096 + 128 * i + 2 * lane);
  SB0;
  STAMP(16);
  f32x4 kq[4];
  u32 kro[4];
  {
    const cf B = {dt * bf_lo(prm[2]), dt * bf_lo(prm[3])}, Cc = {bf_lo(prm[4]), bf_lo(prm[5])};
    const float a2 = (dt * bf_lo(prm[0])) * 1.4426950408889634f, b2 = (dt * bf_lo(prm[1])) * 0.15915494309189535f;
    cf v = cmul(Cc, B);
    u32 we = __builtin_bit_cast(u32, wave_max(__builtin_fmaxf(__builtin_fabsf(v.x), __builtin_fabsf(v.y)))) >> 23;
    we = we < 1 ? 1 : we > 253 ? 253 : we;
    const float wsc = __builtin_bit_cast(float, (254 - we) << 23), wun = __builtin_bit_cast(float, we << 23);
    v = {v.x * wsc, v.y * -wsc};
    const cf z1 = cexp2pi(a2, -b2), z16 = cexp2pi(16.f * a2, -16.f * b2), z256 = cexp2pi(256.f * a2, 256.f * b2);
    LAS char* wr = lds + tbl + 4 * lane;
#define KPUT(row, v) *(LAS h2*)(wr + 288 * (row)) = pkh(v)
    cf l1[4], l2[4], l3[4], l4[4];
    cmuln<4>(l1, {z1, z256, v, z16}, {z1, z256, z16, z16});
    KPUT(14, (cf{z1.x, -z1.y})); KPUT(11, z256); KPUT(4, v);
    SBV;
    cmuln<4>(l2, {l1[0], l1[1], l1[2], l1[3]}, {l1[0], l1[1], z16, l1[3]});
    KPUT(15, (cf{l1[0].x, -l1[0].y})); KPUT(12, l1[1]); KPUT(5, l1[2]);
    SBV;
    cmuln<4>(l3, {l2[0], l2[1], l2[2], l2[3]}, {l2[0], l2[1], z16, l2[3]});
    KPUT(1, l2[0]); KPUT(17, l2[1]); KPUT(6, l2[2]); KPUT(8, (cf{l2[3].x, -l2[3].y}));
    SBV;
    cmuln<4>(l4, {l1[0], l1[1], l3[0], l3[3]}, {z1, z256, l2[0], l2[3]});
    KPUT(2, l3[0]); KPUT(7, l3[2]); KPUT(9, (cf{l3[3].x, -l3[3].y}));
    const float kl = wave_sum63(v.x * l3[1].x + v.y * l3[1].y) * wun;
    SBV;
    KPUT(16, (cf{l4[0].x, -l4[0].y})); KPUT(13, l4[1]); KPUT(3, l4[2]); KPUT(10, (cf{l4[3].x, -l4[3].y}));
    STAMP(17);
    f32x4 k0 = {}, k1 = {}, k2 = {};
    u32x4 x[2][7];
#pragma unroll
    for (int j = 0; j < 7; ++j) x[0][j] = *(LAS u32x4*)(lds + ra[j]);
#pragma unroll
    for (int ks = 0; ks < 4; ++ks) {
      const u32x4(&c)[7] = x[ks & 1];
      SB0;
      if (ks < 3) {
#pragma unroll
        for (int j = 0; j < 7; ++j) x[(ks + 1) & 1][j] = *(LAS u32x4*)(lds + ra[j] + 64 * (ks + 1));
      }
      if (ks == 2) g1_dma<1, 0>(d);
      if (ks == 3) g1_dma<1, 1>(d);
      SB0;
      const u32x4 t0 = cmulh4(c[2], c[3]);
      SB0;
      const u32x4 tc = cmulh4(c[4], c[3]);
      SB0;
      const u32x4 fa = cmulh4(c[0], c[1]);
      if (ks == 2) g1_dma<1, 2>(d);
      if (ks == 3) g1_dma<1, 3>(d);
      SB0;
      const u32x4 t1 = cmulh4(t0, c[6]);
      SB0;
      MFH(k0, H8(fa), H8(t0));
      SB0;
      const u32x4 t2 = cmulh4(tc, c[5]);
      if (ks == 2) g1_dma<1, 4>(d);
      if (ks == 3) g1_dma<1, 5>(d);
      SB0;
      MFH(k1, H8(fa), H8(t1));
      asm volatile("s_nop 0");
      SB0;
      MFH(k2, H8(fa), H8(t2));
    }
    STAMP(18);
    f32x4 ks4[4] = {k0 * wun, k1 * wun, k2 * wun, f32x4{kl, 0.f, 0.f, 0.f}};
    u32 ko4[4];
    const u32 kb = LD_KR + KR_STRIDE * ch, o = 64 * r16 + 16 * (q ^ ((r16 >> 1) & 3));
    ko4[0] = kb + o; ko4[1] = kb + o + 1024; ko4[2] = kb + o + 2560; ko4[3] = kb + (lane == 63 ? 2048 : 2176 + 16 * (lane & 7));
    asm volatile("" : "+v"(ks4[0]), "+v"(ks4[1]), "+v"(ks4[2]), "+v"(ks4[3]), "+v"(ko4[0]), "+v"(ko4[1]), "+v"(ko4[2]), "+v"(ko4[3]));
    const bool w1 = wave & 1, w2 = wave & 2;
    f32x4 kr1[4];
    u32 ko1[4];
#pragma unroll
    for (int i = 0; i < 4; ++i) { kr1[i] = w1 ? ks4[(i + 1) & 3] : ks4[i]; ko1[i] = w1 ? ko4[(i + 1) & 3] : ko4[i]; }
    asm volatile("" : "+v"(kr1[0]), "+v"(kr1[1]), "+v"(kr1[2]), "+v"(kr1[3]), "+v"(ko1[0]), "+v"(ko1[1]), "+v"(ko1[2]), "+v"(ko1[3]));
#pragma unroll
    for (int i = 0; i < 4; ++i) { kq[i] = w2 ? kr1[(i + 2) & 3] : kr1[i]; kro[i] = w2 ? ko1[(i + 2) & 3] : ko1[i]; }
  }
  asm volatile("" : "+s"(tag));
  tag += 1;
  STAMP(19);
  wait_vm<6>();
  BARL;
  G1Frag fr[3];
  g1_rd<0, 0, 0>(fr[0], lds, d); g1_rd<0, 0, 1>(fr[0], lds, d); g1_rd<0, 0, 2>(fr[0], lds, d);
  g1_rd<0, 1, 0>(fr[1], lds, d); g1_rd<0, 1, 1>(fr[1], lds, d); g1_rd<0, 1, 2>(fr[1], lds, d);
  STAMP(1);

  s16x8 wdf[7][4];
  u32x4 urow;
  f32x16 acc[2][2] = {};
  LAS char* cp = lds + LD_CP + CP_STRIDE * ch;
  LAS char* upl = lds + LD_UP + UP_STRIDE * ch;
  {
    u64 fu;
    u32x4 klo, khi;
    f32x4 kt;
    float kg[8];
    u32 aa[7], cb0, zo;
    const char* fup = P.ws + WS_FW + 128 * (((7 * wave) >> 1) + ((lane & 3) < 3 ? (lane & 3) : ((7 * wave + 6) >> 1) - ((7 * wave) >> 1)));
    const char* fgp = P.ws + WS_FG + 128 * (lane & 31);
    u64 fg;
    auto extra = [&](auto I) {
      constexpr int X = decltype(I)::v, S = X >= 1000 ? 99 : X >> 2, J = X & 3;
      constexpr int SLW = 17;
      if constexpr (J == 0 && (S & 3) == 2 && S < 26) {
        constexpr int nlw = LW_STEP * (S - SLW) < LW_EARLY ? LW_STEP * (S - SLW) : LW_EARLY;
        constexpr int N = S == 14 ? 6 + 1 : S == 18 ? 63 : S == 22 ? nlw : 6;
        asm volatile("s_waitcnt vmcnt(%0) lgkmcnt(0)\n\ts_barrier" :: "n"(N) : "memory");
      }
      if constexpr ((S == 1 || S == 5) && J == 1) {
        constexpr int st = S == 1 ? 1 : 2;
#pragma unroll
        for (int i = 0; i < 4; ++i) { d.xa[st][i] = d.xa[0][i] + 49152 * st; asm volatile("" : "+v"(d.xa[st][i])); }
#pragma unroll
        for (int i = 0; i < 2; ++i) { d.wa[st][i] = d.wa[0][i] + 49152 * st; asm volatile("" : "+v"(d.wa[st][i])); }
      }
      if constexpr (S < 14) {
        constexpr int T = (S + 2) >> 2, PH = S < 2 ? S + 2 : (S + 2) & 3;
        if constexpr (T > 0 && PH == 1 && J == 2) kt = *(LAS f32x4*)(lds + kro[T]);
        if constexpr (PH == 3 && J == 2) *(LAS f32x4*)(lds + kro[T]) = T > 0 ? add4(kq[T], kt) : kq[T];
      }
      if constexpr (S == 3 && J == 3) { if (tid < 3) *(LAS u32*)(lds + SY_HCNT + 4 * tid) = 0; }
      if constexpr ((ABL & 4) ? (S >= 14 && S < 18 && J == 1) : ((S == 7 || S == 9 || S == 11 || S == 13) && J == 1)) {
        constexpr int i = (ABL & 4) ? S - 14 : (S - 7) >> 1;
        const u32 l = 2047 - 8 * tl - 2 * i;
        const u32 b2 = pk_bf16((float)l, (float)(l - 1));
        const u32 v0 = (u32)bf_lo(b2), v1 = (u32)bf_hi(b2);
        const u32 o0 = (v0 & ~3u) + 256 * ((0x0d0a0c00u >> (8 * (v0 & 3))) & 255u);
        const u32 o1 = (v1 & ~3u) + 256 * ((0x0d0a0c00u >> (8 * (v1 & 3))) & 255u);
        kofs[2 * i] = LD_KR + KR_STRIDE * ch + (o0 ^ ((o0 >> 3) & 48u));
        kofs[2 * i + 1] = LD_KR + KR_STRIDE * ch + (o1 ^ ((o1 >> 3) & 48u));
        asm volatile("" : "+v"(kofs[2 * i]), "+v"(kofs[2 * i + 1]));
      }
      if constexpr ((S == 15 || S == 16) && J >= 2) {
        constexpr int j = 2 * (S - 15) + J - 2;
        kg[2 * j] = *(LAS float*)(lds + kofs[2 * j]);
        kg[2 * j + 1] = *(LAS float*)(lds + kofs[2 * j + 1]);
      }
      if constexpr (S == 19 && J == 3)
        *(LAS u32x4*)(lds + LD_RK + 4096 * ch + 16 * tl) =
            u32x4{pk_bf16(kg[0], kg[1]), pk_bf16(kg[2], kg[3]), pk_bf16(kg[4], kg[5]), pk_bf16(kg[6], kg[7])};
      if constexpr (S == 13 && J == 1) asm volatile("global_load_dwordx2 %0, %1, off sc1" : "=v"(fu) : "v"(fup) : "memory");
      if constexpr (S == SLW && J == 0) {
        asm volatile("s_waitcnt vmcnt(6)" : "+v"(fu) :: "memory");
        if (__builtin_expect(!all_eq(fu, tag), 0)) wait_flags(fup, tag);
        asm volatile("" ::: "memory");
      }
      if constexpr (S == 22 && J == 2) fg = ld_flag(fgp);
      if constexpr (S == 25 && J == 0) {
        if (__builtin_expect(!all_eq(fg, tag), 0)) wait_flags(fgp, tag);
        asm volatile("" ::: "memory");
        urow = __builtin_amdgcn_raw_buffer_load_b128(rws, 16 * tl, WS_UT + (d0 + ch) * TS, 16);
      }
      if constexpr (S >= SLW && J >= 1 && J <= LW_STEP) {
        constexpr int k = LW_STEP * (S - SLW) + J - 1;
        if constexpr (k < LW_EARLY) LDWD(k);
      }
      if constexpr (S == 22 && J == 1) {
        klo = *(LAS u32x4*)(lds + LD_RK + 4096 * ch + 16 * (255 - tl));
        khi = *(LAS u32x4*)(lds + LD_RK + 4096 * ch + 16 * (tl ? 256 - tl : 0));
      }
      if constexpr (S == 22 && J == 3) {
        if (tl < 196) *(LAS u32x4*)(lds + zo) = u32x4{0, 0, 0, 0};
      }
      if constexpr (S == 21 && J == 1) {
        zo = LD_CP + CP_STRIDE * ch + 16 * tl;
        zo += tl >= 152 ? 275 * 128 - 16 * 152 : 0;
        asm volatile("" : "+v"(zo));
        zo += tl >= 160 ? (LD_UP - LD_CP) - (CP_STRIDE - UP_STRIDE) * ch - 275 * 128 - 16 * 8 : 0;
        asm volatile("" : "+v"(zo));
        zo += tl >= 192 ? 4608 - 16 * 32 : 0;
        asm volatile("" : "+v"(zo));
      }
      if constexpr (S == 23 && J == 1) {
        if (tl == 0) khi = u32x4{0, 0, 0, 0};
        const u32 dd[8] = {klo[0], klo[1], klo[2], klo[3], khi[0], khi[1], khi[2], khi[3]};
#pragma unroll
        for (int k = 0; k < 7; ++k) aa[k] = __builtin_amdgcn_alignbit(dd[k + 1], dd[k], 16);
        cb0 = 128 * (tl + 19) + 16 * ((tl + 19) & 7);
      }
      if constexpr (S >= 23 && S < 28 && (J == 1 || J == 3) && 2 * (S - 23) + (J >> 1) >= 1) {
        constexpr int sh = 2 * (S - 23) + (J >> 1) - 1;
        if constexpr (sh < 8) {
          constexpr int o = (7 - sh) >> 1;
          const u32 dd[8] = {klo[0], klo[1], klo[2], klo[3], khi[0], khi[1], khi[2], khi[3]};
          *(LAS u32x4*)(cp + (cb0 ^ (16 * sh))) =
              (sh & 1) ? u32x4{dd[o], dd[o + 1], dd[o + 2], dd[o + 3]} : u32x4{aa[o], aa[o + 1], aa[o + 2], aa[o + 3]};
        }
      }
    };
    g1_chunk<0>(acc, fr, lds, d, extra);
    STAMP(2);
    g1_chunk<1>(acc, fr, lds, d, extra);
    STAMP(27);
    g1_chunk<2>(acc, fr, lds, d, extra);
    STAMP(28);
    g1_chunk<3>(acc, fr, lds, d, extra);
    STAMP(3);
    g1_chunk<4>(acc, fr, lds, d, extra);
    STAMP(29);
    g1_chunk<5>(acc, fr, lds, d, extra);
    STAMP(20);
    g1_chunk<6>(acc, fr, lds, d, extra);
  }
  asm volatile("s_nop 7\n\ts_nop 7");
  STAMP(4);
  {
    const u32 hv = WS_H + ((u32)(tb * 8 + 4 * wt + (q & 1)) * 56 + fb * 4 + wf) * 1024 + hh * 256 + r16 * 16;
#pragma unroll
    for (int gi = 0; gi < 4; ++gi) {
      const int tt = gi >> 1, s = gi & 1;
      f32x2 g2[4], u2[4], e[4], gv[4], hv2[4];
#pragma unroll
      for (int j = 0; j < 4; ++j) {
        g2[j] = f32x2{acc[0][tt][8 * s + 2 * j], acc[0][tt][8 * s + 2 * j + 1]};
        u2[j] = f32x2{acc[1][tt][8 * s + 2 * j], acc[1][tt][8 * s + 2 * j + 1]};
      }
      pk4_mul_s(e, g2, f32x2{-1.4426950408889634f, -1.4426950408889634f});
      SBV;
      pk4_mul(gv, g2, u2);
      SBV;
#pragma unroll
      for (int j = 0; j < 4; ++j) e[j] = f32x2{__builtin_amdgcn_exp2f(e[j].x), __builtin_amdgcn_exp2f(e[j].y)};
      SBV;
      pk4_add1(e);
      SBV;
#pragma unroll
      for (int j = 0; j < 4; ++j) e[j] = f32x2{__builtin_amdgcn_rcpf(e[j].x), __builtin_amdgcn_rcpf(e[j].y)};
      SBV;
      asm volatile("s_nop 0");
      pk4_mul(hv2, gv, e);
      SBV;
      const u32x4 o = {pk_bf16(hv2[0]), pk_bf16(hv2[1]), pk_bf16(hv2[2]), pk_bf16(hv2[3])};
      SBV;
      __builtin_amdgcn_raw_buffer_store_b128(o, rws, hv + tt * 2 * 56 * 1024 + s * 512, 0, 16);
    }
  }
  *(LAS u32x4*)(upl + 512 + 16 * tl) = urow;
  STAMP(5);
  BARL;
  STAMP(6);
  const int j0 = (7 * wave) >> 2, j1 = (7 * wave + 6) >> 2;
  const char* fhp = P.ws + WS_FH + 128 * ((rb >> 1) * 14 + (j0 + (lane & 3) < j1 ? j0 + (lane & 3) : j1));
  u64 fh;
    s16x8 hf[28];
    const u32 hs = WS_H + ((u32)rb * 4 * 56 + 7 * wave) * 1024;
#define LDH(i) hf[i] = __builtin_bit_cast(s16x8, __builtin_amdgcn_raw_buffer_load_b128(rws, lane * 16, hs + (((i) / 7) * 56 + (i) % 7) * 1024, 16))
  {
    const int cj = (wave + ch) & 3;
    f32x4 ya[8] = {};
    u32 ab[2], bb[2];
#pragma unroll
    for (int i = 0; i < 2; ++i) {
      const int rho = 2 * cj + i;
      const int sft = rho >= 5;
      const int bq = 16 * ((lane >> 3) & 1) - q + 19 - 4 * rho + 32 * sft;
      ab[i] = LD_CP + CP_STRIDE * ch + 128 * bq + 16 * ((lane & 7) ^ (bq & 7));
      bb[i] = LD_UP + UP_STRIDE * ch + 512 + 16 * (r16 + q) + 64 * rho - 512 * sft;
      asm volatile("" : "+v"(ab[i]), "+v"(bb[i]));
    }
#define RDA(i, e) *(LAS s16x8*)(lds + ab[i] + 4096 * (e))
#define RDB(i, p) *(LAS s16x8*)(lds + bb[i] + 512 * (p))
    s16x8 a0[8], b0[8], a1[8], b1[8];
#pragma unroll
    for (int e = 7; e >= 0; --e) { a0[e] = RDA(0, e); b0[7 - e] = RDB(0, 7 - e); }
    SB0;
#pragma unroll
    for (int e = 7; e >= 0; --e) {
#pragma unroll
      for (int jt = e; jt < 8; ++jt) MF16(ya[jt], a0[e], b0[jt - e]);
      SB0;
      a1[7 - e] = RDA(1, 7 - e);
      if (e < HPUB && LW_EARLY + 2 * (HPUB - 1 - e) + 1 < 28) { LDWD(LW_EARLY + 2 * (HPUB - 1 - e)); LDWD(LW_EARLY + 2 * (HPUB - 1 - e) + 1); }
      if (e == HPUB) {
        STAMP(21);
        asm volatile("s_waitcnt vmcnt(0)" ::: "memory");
        STAMP(22);
        if (lds_add(lds, SY_HCNT, lane) == 7 && lane == 0) st_flag(P.ws + WS_FH + 128 * mid, tag);
      }
      SB0;
    }
    if (cj == 2) {
      const s16x8 bm = *(LAS s16x8*)(lds + bb[0] - 512);
#pragma unroll
      for (int jt = 0; jt < 7; ++jt) MF16(ya[jt], a0[jt + 1], bm);
      MF16(ya[7], RDA(0, 8), bm);
    }
    SB0;
#pragma unroll
    for (int p = 0; p < 8; ++p) b1[p] = RDB(1, p);
    SB0;
    const int wj = wave & 3;
#pragma unroll
    for (int jt = 0; jt < 8; ++jt) {
#pragma unroll
      for (int e = 0; e <= jt; ++e) MF16(ya[jt], a1[e], b1[jt - e]);
      SB0;
#pragma unroll
      for (int k = 0; k < 2; ++k)
        if (LW_EARLY + 2 * HPUB + 2 * jt + k < 28) LDWD(LW_EARLY + 2 * HPUB + 2 * jt + k);
      if (jt == FHP) fh = ld_flag(fhp);
      if (jt > 0) {
        const int dj = (jt - 1) >> 1;
        *(LAS f32x4*)(lds + LD_EX + 8192 * (4 * ch + dj) + 2048 * ((wj - dj) & 3) + 1024 * ((jt - 1) & 1) + 16 * lane) = ya[jt - 1];
      }
      SB0;
    }
    *(LAS f32x4*)(lds + LD_EX + 8192 * (4 * ch + 3) + 2048 * ((wj - 3) & 3) + 1024 + 16 * lane) = ya[7];
    STAMP(7);
    if (__builtin_expect(!all_eq(fh, tag), 0)) wait_flags(fhp, tag);
    asm volatile("" ::: "memory");
    STAMP(9);
#pragma unroll
    for (int i = 0; i < G2_PRE0; ++i) LDH(i);
    STAMP(26);
    BARL;
    const float dsk = ch ? bf_hi(p_dsk) : bf_lo(p_dsk);
    const u32 tq = 128 * (q >> 1) + 8 * r16 + 4 * (q & 1) + 512 * wj;
    f32x4 y[2];
    u32x2 uu[2];
#pragma unroll
    for (int j = 0; j < 2; ++j) {
      LAS char* ex = lds + LD_EX + 8192 * wave + 1024 * j + 16 * lane;
      y[j] = (*(LAS f32x4*)ex + *(LAS f32x4*)(ex + 2048)) + (*(LAS f32x4*)(ex + 4096) + *(LAS f32x4*)(ex + 6144));
      uu[j] = *(LAS u32x2*)(upl + 512 + 2 * (tq + 256 * j));
    }
#pragma unroll
    for (int j = 0; j < 2; ++j) {
      const u32 y0 = pk_bf16(y[j][0], y[j][1]), y1 = pk_bf16(y[j][2], y[j][3]);
      const u32 s0 = pk_bf16(dsk * bf_lo(uu[j][0]), dsk * bf_hi(uu[j][0])), s1 = pk_bf16(dsk * bf_lo(uu[j][1]), dsk * bf_hi(uu[j][1]));
      const u32x2 o = {pk_bf16(bf_lo(y0) + bf_lo(s0), bf_hi(y0) + bf_hi(s0)), pk_bf16(bf_lo(y1) + bf_lo(s1), bf_hi(y1) + bf_hi(s1))};
      __builtin_amdgcn_raw_buffer_store_b64(o, rws, 2 * tq, WS_AT + (d0 + ch) * TS + 512 * j, 16);
    }
    SB0;
#pragma unroll
    for (int i = G2_PRE0; i < G2_PRE; ++i) LDH(i);
    lds_add(lds, SY_XCNT, lane);
  }
  STAMP(8);
  {
    const char* fap = P.ws + WS_FA + 128 * (32 * cb + 4 * wave + (lane & 3));
    u64 fa;
    u32x4 avl;
    u32 gv;
    LAS char* pp = lds + 16384 * wave + 256 * r16;
    bool aok = false;
    SB0;
#pragma unroll
    for (int tt = 0; tt < 4; ++tt) {
      f32x4 oa[4] = {};
      if (tt == 3) {
        aok = all_eq(fa, tag);
        asm volatile("" ::: "memory");
        if (__builtin_expect(aok, 1)) {
          avl = __builtin_amdgcn_raw_buffer_load_b128(rws, (u32)(64 * cb + (tid >> 3)) * TS + 16 * (tid & 7), WS_AT + 128 * rb, 16);
          gv = __builtin_amdgcn_raw_buffer_load_b32(rws, 4 * (64 * rb + lane), WS_G, 16);
        }
      }
      SB0;
#pragma unroll
      for (int k = 0; k < 7; ++k) {
#pragma unroll
        for (int dt4 = 0; dt4 < 4; ++dt4) MF16(oa[dt4], wdf[k][dt4], hf[7 * tt + k]);
        SB0;
        if (7 * tt + k + G2_PRE < 28) LDH(7 * tt + k + G2_PRE);
        if (7 * tt + k + G2_PRE == 28) fa = ld_flag(fap);
        if (7 * tt + k == APUB) {
          wait_vm<APUB + 1 + G2_PRE - G2_PRE0>();
          if (lds_add(lds, SY_ACNT, lane) == 7 && lane == 0) st_flag(P.ws + WS_FA + 128 * mid, tag);
        }
        SB0;
      }
      if (tt == 0) {
        u32 xc = __builtin_amdgcn_readfirstlane(*(volatile LAS u32*)(lds + SY_XCNT));
        if (__builtin_expect(xc < 8, 0)) lds_wait_ge(lds, SY_XCNT, 8);
        STAMP(23);
      }
      if (tt == 1) STAMP(24);
      if (tt == 2) STAMP(25);
#pragma unroll
      for (int dt4 = 0; dt4 < 4; ++dt4) *(LAS f32x4*)(pp + 4096 * tt + 16 * ((4 * dt4 + q) ^ r16)) = oa[dt4];
      SB0;
    }
    STAMP(10);
    if (__builtin_expect(!aok, 0)) {
      wait_flags(fap, tag);
      avl = __builtin_amdgcn_raw_buffer_load_b128(rws, (u32)(64 * cb + (tid >> 3)) * TS + 16 * (tid & 7), WS_AT + 128 * rb, 16);
      gv = __builtin_amdgcn_raw_buffer_load_b32(rws, 4 * (64 * rb + lane), WS_G, 16);
    }
    {
      *(LAS u32x4*)(lds + LD_AT + 144 * (tid >> 3) + 16 * (tid & 7)) = avl;
      if (wave == 0) *(LAS u32*)(lds + LD_AT + 9216 + 4 * lane) = gv;
    }
    STAMP(11);
    BARL;
    STAMP(12);
    const int dh = wave & 1;
    const int t = 64 * rb + 16 * (wave >> 1) + r16;
    f32x4 b0 = {}, b1 = {};
    const u32 ro = 4096 * (wave >> 1) + 256 * r16;
    const u32 r0 = ro + 16 * ((8 * dh + 2 * q) ^ r16), r1 = ro + 16 * ((8 * dh + 2 * q + 1) ^ r16);
    u32 r0h = r0 + 65536, r1h = r1 + 65536;
    asm volatile("" : "+v"(r0h), "+v"(r1h));
#pragma unroll
    for (int w = 0; w < 4; ++w) {
      b0 += *(LAS f32x4*)(lds + 16384 * w + r0) + *(LAS f32x4*)(lds + 16384 * w + r0h);
      b1 += *(LAS f32x4*)(lds + 16384 * w + r1) + *(LAS f32x4*)(lds + 16384 * w + r1h);
    }
    const u32 aad = LD_AT + 144 * (32 * dh + 8 * q + (r16 >> 2)) + 32 * (wave >> 1) + 8 * (lane & 3);
    const u32x4 av = __builtin_bit_cast(u32x4, tr_frag(lds, aad, aad + 4 * 144));
    const float g = *(LAS float*)(lds + LD_AT + 9216 + 4 * (16 * (wave >> 1) + r16));
    const float og = rbf(1.f - g);
    const u32 bb[4] = {pk_bf16(b0[0], b0[1]), pk_bf16(b0[2], b0[3]), pk_bf16(b1[0], b1[1]), pk_bf16(b1[2], b1[3])};
    u32x4 o;
#pragma unroll
    for (int j = 0; j < 4; ++j) {
      const u32 x = pk_bf16(g * bf_lo(av[j]), g * bf_hi(av[j])), y = pk_bf16(og * bf_lo(bb[j]), og * bf_hi(bb[j]));
      o[j] = pk_bf16(bf_lo(x) + bf_lo(y), bf_hi(x) + bf_hi(y));
    }
    *(u32x4*)(P.out + (size_t)t * D_ + 64 * cb + 32 * dh + 8 * q) = o;
    STAMP(13);
    STAMP_OUT;
  }
}

extern "C" void kernel_launch(void* const* d_in, const int* in_sizes, int n_in, void* d_out, int out_size, void* d_ws,
                              size_t ws_size, hipStream_t stream) {
  Params P;
  P.u = (const u16*)d_in[0];
  P.are = (const u16*)d_in[1];
  P.aim = (const u16*)d_in[2];
  P.bre = (const u16*)d_in[3];
  P.bim = (const u16*)d_in[4];
  P.cre = (const u16*)d_in[5];
  P.cim = (const u16*)d_in[6];
  P.ldt = (const u16*)d_in[7];
  P.dsk = (const u16*)d_in[8];
  P.wg = (const u16*)d_in[10];
  P.wu = (const u16*)d_in[11];
  P.wd = (const u16*)d_in[12];
  P.gw = (const u16*)d_in[13];
  P.out = (u16*)d_out;
  P.ws = (char*)d_ws;
  gated_mix_kernel<<<256, 512, 0, stream>>>(P);
}
```
